```python
import torch

def _softmax(x, axis=-1):
    return torch.softmax(x, dim=axis)
def _gelu(x):
    return torch.nn.functional.gelu(x, approximate="tanh")
def _silu(x):
    return torch.nn.functional.silu(x)
def _sigmoid(x):
    return torch.sigmoid(x)
def _layernorm(x, axis=-1, eps=1e-5):
    m = x.mean(dim=axis, keepdim=True)
    v = x.var(dim=axis, keepdim=True, unbiased=False)
    return (x - m) / torch.sqrt(v + eps)
def _scatter_add(tgt, idx, src, axis=0):
    return tgt.clone().index_add_(axis, idx, src)
def _segment_sum(src, seg, n):
    out = torch.zeros((n,) + src.shape[1:], dtype=src.dtype)
    return out.index_add_(0, seg, src)
def _pad(x, pw):
    flat = tuple(v for pair in reversed(pw) for v in pair)
    return torch.nn.functional.pad(x, flat)

kernel_name = 'recurrent_scan__grouped_kv_mqa__swiglu_ffn__prompt'

def setup_inputs():

    _w = (lambda a: (lambda t: t.to(torch.bfloat16) if t.is_floating_point() else t)(torch.as_tensor(a)))
    def _A_setup():
        B, S, D, H = 1, 2048, 640, 32
        Dh = D // H
        return {
            "x": _w((lambda *s: torch.randn(*s, dtype=torch.bfloat16) * 0.05)(B, S, D)),
            "wq": _w((lambda *s: torch.randn(*s, dtype=torch.bfloat16) * 0.05)(D, H * Dh)),
            "wk": _w((lambda *s: torch.randn(*s, dtype=torch.bfloat16) * 0.05)(D, Dh)),
            "wv": _w((lambda *s: torch.randn(*s, dtype=torch.bfloat16) * 0.05)(D, Dh)),
            "wo": _w((lambda *s: torch.randn(*s, dtype=torch.bfloat16) * 0.05)(H * Dh, D)),
        }
    _A_in = _A_setup()

    def _B_setup():
        B, S, D, F = 1, 2048, 640, 1728
        return {
            "x": _w((lambda *s: torch.randn(*s, dtype=torch.bfloat16) * 0.05)(B, S, D)),
            "w_gate": _w((lambda *s: torch.randn(*s, dtype=torch.bfloat16) * 0.05)(D, F)),
            "w_up": _w((lambda *s: torch.randn(*s, dtype=torch.bfloat16) * 0.05)(D, F)),
            "w_down": _w((lambda *s: torch.randn(*s, dtype=torch.bfloat16) * 0.05)(F, D)),
        }
    _B_in = _B_setup()

    return {
        "A_x": _A_in["x"],
        "A_wq": _A_in["wq"],
        "A_wk": _A_in["wk"],
        "A_wv": _A_in["wv"],
        "A_wo": _A_in["wo"],
        "B_x": _B_in["x"],
        "B_w_gate": _B_in["w_gate"],
        "B_w_up": _B_in["w_up"],
        "B_w_down": _B_in["w_down"],
    }

def reference(A_x, A_wq, A_wk, A_wv, A_wo, B_x, B_w_gate, B_w_up, B_w_down):
    def _A_ref(x, wq, wk, wv, wo):
        B, S, D = x.shape
        H = 32
        Dh = D // H
        q = (lambda x, *d: x.permute(*d))((x @ wq).reshape(B, S, H, Dh), 0, 2, 1, 3)
        k = x @ wk
        v = x @ wv
        scores = torch.einsum("bhqd,bkd->bhqk", q, k) * (Dh ** -0.5)
        p = _softmax(scores, axis=-1)
        o = torch.einsum("bhqk,bkd->bhqd", p, v)
        return (lambda x, *d: x.permute(*d))(o, 0, 2, 1, 3).reshape(B, S, H * Dh) @ wo

    def _B_ref(x, w_gate, w_up, w_down):
        g = _silu(x @ w_gate)
        u = x @ w_up
        return (g * u) @ w_down

    x = A_x
    xn = _layernorm(x, axis=-1)
    s = _A_ref(xn, A_wq, A_wk, A_wv, A_wo)
    g = _silu(_B_ref(xn, B_w_gate, B_w_up, B_w_down))
    return x + s * g
```

```cpp
#include <hip/hip_runtime.h>
#include <hip/hip_bf16.h>
#include <stdint.h>

typedef __bf16 bf16x8 __attribute__((ext_vector_type(8)));
typedef __bf16 bf16x16 __attribute__((ext_vector_type(16)));
typedef __bf16 bf16x2 __attribute__((ext_vector_type(2)));
typedef short s16x4 __attribute__((ext_vector_type(4)));
typedef float f32x16 __attribute__((ext_vector_type(16)));
typedef float f32x8 __attribute__((ext_vector_type(8)));
typedef float f32x4 __attribute__((ext_vector_type(4)));
typedef float f32x2 __attribute__((ext_vector_type(2)));
typedef unsigned u32x4 __attribute__((ext_vector_type(4)));
typedef unsigned u32x2 __attribute__((ext_vector_type(2)));
#define LAS __attribute__((address_space(3)))
#define DI __device__ __forceinline__

constexpr int S_ = 2048, D_ = 640, F_ = 1728;
constexpr float LOG2E = 1.4426950408889634f;
constexpr float QSCALE = 0.22360679774997896f * LOG2E;

constexpr size_t WS_Q = 0;
constexpr size_t WS_KV = WS_Q + (size_t)S_ * D_ * 2;
constexpr size_t WS_KN = WS_KV + 64 * 4096;
constexpr size_t WS_H = WS_KN + 1024;
constexpr size_t WS_O = WS_H + (size_t)S_ * F_ * 2;
constexpr size_t WS_HS = WS_O + (size_t)S_ * D_ * 2;

DI unsigned cvtpk(float lo, float hi) {
  bf16x2 v = {(__bf16)lo, (__bf16)hi};
  return __builtin_bit_cast(unsigned, v);
}
DI float bflo(unsigned u) { return __uint_as_float(u << 16); }
DI float bfhi(unsigned u) { return __uint_as_float(u & 0xffff0000u); }
DI f32x16 mfma32(u32x4 a, u32x4 b, f32x16 c) {
  return __builtin_amdgcn_mfma_f32_32x32x16_bf16(__builtin_bit_cast(bf16x8, a), __builtin_bit_cast(bf16x8, b), c, 0, 0, 0);
}
DI float dot2(unsigned a, unsigned b, float c) {
  return __builtin_amdgcn_fdot2_f32_bf16(__builtin_bit_cast(bf16x2, a), __builtin_bit_cast(bf16x2, b), c, false);
}
DI u32x2 trread(unsigned addr) {
  return __builtin_bit_cast(u32x2, __builtin_amdgcn_ds_read_tr16_b64_v4i16((LAS s16x4*)(uintptr_t)addr));
}
DI u32x4 ldsread16(unsigned addr) { return *(LAS u32x4*)(uintptr_t)addr; }
DI void glds16(const void* sbase, unsigned voff, unsigned lds) {
  asm volatile("s_mov_b32 m0, %2\n\ts_nop 0\n\tglobal_load_lds_dwordx4 %0, %1" ::"v"(voff), "s"(sbase), "s"(lds) : "memory");
}
DI float xhalf_add(float v) {
  auto r = __builtin_amdgcn_permlane32_swap(__float_as_uint(v), __float_as_uint(v), false, false);
  return __uint_as_float(r[0]) + __uint_as_float(r[1]);
}
DI float xhalf_max(float v) {
  auto r = __builtin_amdgcn_permlane32_swap(__float_as_uint(v), __float_as_uint(v), false, false);
  return fmaxf(__uint_as_float(r[0]), __uint_as_float(r[1]));
}
DI float wave_max(float v) {
  v = xhalf_max(v);
#pragma unroll
  for (int o = 16; o >= 1; o >>= 1) v = fmaxf(v, __shfl_xor(v, o));
  return v;
}
DI int rfl(int v) { return __builtin_amdgcn_readfirstlane(v); }
#define WAITV(n) asm volatile("s_waitcnt vmcnt(" #n ")" ::: "memory")
#define BARRIER() asm volatile("s_waitcnt lgkmcnt(0)\n\ts_barrier" ::: "memory")

constexpr int K1_XOFF = 32768, K1_EOFF = 49152, K1_STAGE = 51712;
constexpr int K1_CS = 3 * K1_STAGE;
constexpr int K1_ST = K1_CS + 9 * 32 * 4;
constexpr int K1_LDS = K1_ST + 2 * 2 * 128 * 4;

struct K1Ctx {
  const char* wbase; const char* xbase; const char* ebase;
  unsigned wv[4], xv[2], ev;
  unsigned wstep;
  unsigned lds0; int w; bool loadx; int c0;
};

DI void k1_issue(const K1Ctx& c, int cc  , int buf) {
  int ch = cc + c.c0; if (ch >= 10) ch -= 10;
  const char* wb = c.wbase + (size_t)ch * c.wstep;
  const char* xb = c.xbase + ch * 128;
  unsigned l = c.lds0 + buf * K1_STAGE;
#pragma unroll
  for (int i = 0; i < 4; ++i) glds16(wb, c.wv[i], l + c.w * 4096 + i * 1024);
#pragma unroll
  for (int i = 0; i < 2; ++i) glds16(xb, c.xv[i], l + K1_XOFF + c.w * 2048 + i * 1024);
  if (c.loadx) {
    if ((threadIdx.x & 63) < 20) glds16(c.ebase + ch * 2560, c.ev, l + K1_EOFF + c.w * 320);
  }
}

template <int EXTRA>
DI void k1_main(const K1Ctx& c, unsigned aAe0, unsigned aAe1, unsigned aAo0, unsigned aAo1, unsigned aB0, unsigned bflip, unsigned aEe, unsigned aEo,
                f32x16 (&acc)[2][2], f32x16& acce, float& csum, float& csume, float& tsum, float& tsq) {
  const unsigned ONES = 0x3f803f80u;
  int buf = 0;
  for (int cc = 0; cc < 10; ++cc) {
    if (cc < 9) WAITV(6); else WAITV(0);
    BARRIER();
    if (cc + 2 < 10) { int b2 = buf + 2; if (b2 >= 3) b2 -= 3; k1_issue(c, cc + 2, b2); }
    const unsigned sb = buf * K1_STAGE;
#pragma unroll
    for (int s = 0; s < 4; ++s) {
      const unsigned a0 = ((s & 1) ? aAo0 : aAe0) + sb + s * 2048, a1 = ((s & 1) ? aAo1 : aAe1) + sb + s * 2048;
      u32x2 p0 = trread(a0), p1 = trread(a0 + 512), p2 = trread(a1), p3 = trread(a1 + 512);
      u32x4 A0 = {p0[0], p0[1], p1[0], p1[1]}, A1 = {p2[0], p2[1], p3[0], p3[1]};
      const unsigned b0 = ((aB0 + sb) ^ (s << 5));
      u32x4 B0 = ldsread16(b0), B1 = ldsread16(b0 + bflip);
      acc[0][0] = mfma32(A0, B0, acc[0][0]);
      acc[1][0] = mfma32(A1, B0, acc[1][0]);
      acc[0][1] = mfma32(A0, B1, acc[0][1]);
      acc[1][1] = mfma32(A1, B1, acc[1][1]);
      if (EXTRA) {
        const unsigned ae = ((s & 1) ? aEo : aEe) + sb + s * 640;
        u32x2 e0 = trread(ae), e1 = trread(ae + 160);
        u32x4 E = {e0[0], e0[1], e1[0], e1[1]};
        if (EXTRA == 1) acce = mfma32(E, B0, acce); else acce = mfma32(B0, E, acce);
#pragma unroll
        for (int i = 0; i < 4; ++i) csume = dot2(E[i], ONES, csume);
      }
#pragma unroll
      for (int i = 0; i < 4; ++i) csum = dot2(A0[i], ONES, csum);
      if ((s & 1) == 0) {
#pragma unroll
        for (int i = 0; i < 4; ++i) { tsum = dot2(B0[i], ONES, tsum); tsq = dot2(B0[i], B0[i], tsq); }
      }
    }
    if (++buf == 3) buf = 0;
  }
}

__global__ void __launch_bounds__(512, 2)
k1_proj(const __bf16* __restrict__ x, const __bf16* __restrict__ wq, const __bf16* __restrict__ wk, const __bf16* __restrict__ wv,
        const __bf16* __restrict__ wg, const __bf16* __restrict__ wu, char* __restrict__ ws) {
  extern __shared__ __attribute__((aligned(128))) char smem[];
  const int tid = threadIdx.x, lane = tid & 63;
  const int w = rfl(tid >> 6), wm = w >> 2, wn = w & 3;
  const int bid = blockIdx.x, xcd = bid & 7, loc = bid >> 3;
  const int rp = 8 * (xcd & 1) + (loc & 7), cg = 4 * (xcd >> 1) + (loc >> 3);
  const unsigned lds0 = (unsigned)(uintptr_t)(LAS char*)smem;

  K1Ctx c;
  c.lds0 = lds0; c.w = w; c.loadx = cg >= 14;
  c.c0 = ((loc & 7) + 3 * (loc >> 3)) % 10;
  {
    const int u = 2 * cg + (w >> 2), part = (w >> 1) & 1;
    int ld; const __bf16* base;
    if (u < 27) { ld = F_; base = (part ? wu : wg) + 64 * u; }
    else { ld = D_; base = wq + 128 * (u - 27) + 64 * part; }
    c.wbase = (const char*)(base + (size_t)(32 * (w & 1)) * ld);
    c.wstep = 64 * ld * 2;
    const int ch = lane & 7, sc = (((ch >> 2) ^ ((lane >> 4) & 1)) << 2) | (ch & 3);
#pragma unroll
    for (int i = 0; i < 4; ++i) c.wv[i] = (unsigned)((8 * i + (lane >> 3)) * ld * 2 + sc * 16);
    c.xbase = (const char*)(x + (size_t)(128 * rp + 16 * w) * D_);
#pragma unroll
    for (int i = 0; i < 2; ++i) c.xv[i] = (unsigned)((8 * i + (lane >> 3)) * D_ * 2 + (((lane & 7) ^ ((4 * i + (lane >> 4)) & 7)) << 4));
    c.ebase = (const char*)(cg == 14 ? wk : wv) + 320 * w;
    c.ev = lane * 16;
  }
  k1_issue(c, 0, 0);
  k1_issue(c, 1, 1);

  const int h = lane >> 5, blk = (lane >> 4) & 1, q = (lane & 15) >> 2, p = lane & 3, r32 = lane & 31;
  const int kpar = wn >> 1;
  const unsigned arow = (8 * h + q) * 128 + (((wn & 1) ^ (q >> 1)) << 6) + blk * 32 + p * 8;
  const unsigned aA0 = lds0 + (2 * (wn >> 1) + wm) * 8192 + arow, aA1 = lds0 + (2 * (wn >> 1) + (1 - wm)) * 8192 + arow;
  const unsigned ks = kpar * 2048;
  const int m0 = 64 * wm + 32 * (wn & 1) + r32;
  const unsigned aB0 = (lds0 + K1_XOFF + m0 * 128 + ((h ^ ((r32 >> 1) & 7)) << 4)) ^ (kpar << 5);
  const unsigned bdel = (wn & 1) ? (unsigned)-4096 : 4096u;
  int ec = 4 * blk + p; if (ec > 4) ec = 4;
  const unsigned aE = lds0 + K1_EOFF + (8 * h + q) * 40 + ec * 8;
  const unsigned kse = kpar * 640;

  f32x16 acc[2][2], acce;
#pragma unroll
  for (int i = 0; i < 16; ++i) { acc[0][0][i] = 0.f; acc[0][1][i] = 0.f; acc[1][0][i] = 0.f; acc[1][1][i] = 0.f; acce[i] = 0.f; }
  float csum = 0.f, csume = 0.f, tsum = 0.f, tsq = 0.f;
  const int extra = (cg >= 14 && (wn >> 1) == wm) ? (cg - 13) : 0;
  if (extra == 0) k1_main<0>(c, aA0 + ks, aA1 + ks, aA0 - ks, aA1 - ks, aB0, bdel, aE + kse, aE - kse, acc, acce, csum, csume, tsum, tsq);
  else if (extra == 1) k1_main<1>(c, aA0 + ks, aA1 + ks, aA0 - ks, aA1 - ks, aB0, bdel, aE + kse, aE - kse, acc, acce, csum, csume, tsum, tsq);
  else k1_main<2>(c, aA0 + ks, aA1 + ks, aA0 - ks, aA1 - ks, aB0, bdel, aE + kse, aE - kse, acc, acce, csum, csume, tsum, tsq);

  float* cs = (float*)(smem + K1_CS);
  float* st = (float*)(smem + K1_ST);
  csum = xhalf_add(csum); csume = xhalf_add(csume); tsum = xhalf_add(tsum); tsq = xhalf_add(tsq);
  if (lane < 32) {
    cs[(2 * wn + wm) * 32 + lane] = csum;
    if (extra) cs[8 * 32 + lane] = csume;
    st[(0 * 2 + kpar) * 128 + m0] = tsum;
    st[(1 * 2 + kpar) * 128 + m0] = tsq;
  }
  __syncthreads();
  float mean[2], rstd[2];
#pragma unroll
  for (int b = 0; b < 2; ++b) {
    const int m = m0 ^ (32 * b);
    const float s1 = st[m] + st[128 + m], s2 = st[256 + m] + st[384 + m];
    mean[b] = s1 * (1.f / D_);
    const float var = fmaxf(s2 * (1.f / D_) - mean[b] * mean[b], 0.f);
    rstd[b] = rsqrtf(var + 1e-5f);
  }
  f32x16 cs0, cs1;
#pragma unroll
  for (int g = 0; g < 4; ++g) {
    f32x4 t0 = *(f32x4*)(cs + (2 * wn) * 32 + 8 * g + 4 * h), t1 = *(f32x4*)(cs + (2 * wn + 1) * 32 + 8 * g + 4 * h);
#pragma unroll
    for (int i = 0; i < 4; ++i) { cs0[4 * g + i] = t0[i]; cs1[4 * g + i] = t1[i]; }
  }
  if (wm) {
#pragma unroll
    for (int b = 0; b < 2; ++b) { f32x16 t = acc[0][b]; acc[0][b] = acc[1][b]; acc[1][b] = t; }
  }
  const int u = 2 * cg + (wn >> 1);
  if (u < 27) {
    const int ft = 2 * u + (wn & 1);
#pragma unroll
    for (int b = 0; b < 2; ++b) {
      const int tt = 4 * rp + 2 * wm + ((wn & 1) ^ b);
      const float nm = -mean[b], c1 = -LOG2E * rstd[b], c2 = rstd[b] * rstd[b];
      float hv[16];
#pragma unroll
      for (int i = 0; i < 16; ++i) {
        const float tg = fmaf(nm, cs0[i], acc[0][b][i]), tu = fmaf(nm, cs1[i], acc[1][b][i]);
        const float e = __builtin_amdgcn_exp2f(tg * c1);
        hv[i] = tg * tu * c2 * __builtin_amdgcn_rcpf(1.f + e);
      }
      char* dst = ws + WS_H + ((size_t)(tt * 108 + 2 * ft) * 64 + lane) * 16;
      u32x4 o0 = {cvtpk(hv[0], hv[1]), cvtpk(hv[2], hv[3]), cvtpk(hv[4], hv[5]), cvtpk(hv[6], hv[7])};
      u32x4 o1 = {cvtpk(hv[8], hv[9]), cvtpk(hv[10], hv[11]), cvtpk(hv[12], hv[13]), cvtpk(hv[14], hv[15])};
      *(u32x4*)dst = o0;
      *(u32x4*)(dst + 1024) = o1;
    }
  } else {
    const int n0 = 128 * (u - 27) + 32 * (wn & 1);
#pragma unroll
    for (int b = 0; b < 2; ++b) {
      const int tok = 128 * rp + (m0 ^ (32 * b));
      const float nm = -mean[b], c1 = QSCALE * rstd[b];
      char* dst = ws + WS_Q + ((size_t)tok * D_ + n0 + 4 * h) * 2;
#pragma unroll
      for (int g = 0; g < 4; ++g) {
        u32x2 o0 = {cvtpk(fmaf(nm, cs0[4 * g], acc[0][b][4 * g]) * c1, fmaf(nm, cs0[4 * g + 1], acc[0][b][4 * g + 1]) * c1),
                    cvtpk(fmaf(nm, cs0[4 * g + 2], acc[0][b][4 * g + 2]) * c1, fmaf(nm, cs0[4 * g + 3], acc[0][b][4 * g + 3]) * c1)};
        u32x2 o1 = {cvtpk(fmaf(nm, cs1[4 * g], acc[1][b][4 * g]) * c1, fmaf(nm, cs1[4 * g + 1], acc[1][b][4 * g + 1]) * c1),
                    cvtpk(fmaf(nm, cs1[4 * g + 2], acc[1][b][4 * g + 2]) * c1, fmaf(nm, cs1[4 * g + 3], acc[1][b][4 * g + 3]) * c1)};
        *(u32x2*)(dst + 16 * g) = o0;
        *(u32x2*)(dst + 128 + 16 * g) = o1;
      }
    }
  }
  if (extra == 1) {
    const int tt = 4 * rp + 2 * wm + (wn & 1);
    unsigned pk[6];
#pragma unroll
    for (int g = 0; g < 3; ++g) {
      f32x4 t = *(f32x4*)(cs + 8 * 32 + 8 * g + 4 * h);
      pk[2 * g] = cvtpk(fmaf(-mean[0], t[0], acce[4 * g]) * rstd[0], fmaf(-mean[0], t[1], acce[4 * g + 1]) * rstd[0]);
      pk[2 * g + 1] = cvtpk(fmaf(-mean[0], t[2], acce[4 * g + 2]) * rstd[0], fmaf(-mean[0], t[3], acce[4 * g + 3]) * rstd[0]);
    }
    if (h) { pk[4] = 0; pk[5] = 0; }
    float n2 = 0.f;
#pragma unroll
    for (int i = 0; i < 6; ++i) { n2 = fmaf(bflo(pk[i]), bflo(pk[i]), n2); n2 = fmaf(bfhi(pk[i]), bfhi(pk[i]), n2); }
    n2 = xhalf_add(n2);
    const bool bad = !(n2 == n2);
    n2 = wave_max(n2);
    if (__any(bad)) n2 = __builtin_inff();
    const int rho = (r32 & 19) | ((r32 & 4) << 1) | ((r32 & 8) >> 1);
    char* dst = ws + WS_KV + (size_t)tt * 4096 + (rho + 32 * h) * 16;
    *(u32x4*)dst = u32x4{pk[0], pk[1], pk[2], pk[3]};
    *(u32x4*)(dst + 1024) = u32x4{pk[4], pk[5], 0u, 0u};
    if (lane == 0) ((float*)(ws + WS_KN))[tt] = n2;
  } else if (extra == 2) {
    const int tt = 4 * rp + 2 * wm + (wn & 1);
    const int mb = 64 * wm + 32 * (wn & 1);
    float v[16];
#pragma unroll
    for (int i = 0; i < 16; ++i) {
      const int m = mb + (i & 3) + 8 * (i >> 2) + 4 * h;
      const float s1 = st[m] + st[128 + m], s2 = st[256 + m] + st[384 + m];
      const float mu = s1 * (1.f / D_);
      const float rs = rsqrtf(fmaxf(s2 * (1.f / D_) - mu * mu, 0.f) + 1e-5f);
      v[i] = fmaf(-mu, csume, acce[i]) * rs;
    }
    u32x4 o0 = {cvtpk(v[0], v[1]), cvtpk(v[2], v[3]), cvtpk(v[4], v[5]), cvtpk(v[6], v[7])};
    u32x4 o1 = {cvtpk(v[8], v[9]), cvtpk(v[10], v[11]), cvtpk(v[12], v[13]), cvtpk(v[14], v[15])};
    if (r32 >= 20) {
      const unsigned f = (r32 == 20 || r32 == 24) ? 0x3f803f80u : 0u;
      o0 = u32x4{f, f, f, f}; o1 = o0;
    }
    char* dst = ws + WS_KV + (size_t)tt * 4096 + 2048 + lane * 16;
    *(u32x4*)dst = o0;
    *(u32x4*)(dst + 1024) = o1;
  }
}

constexpr int SG_SLOT = 6144, SG_LDS = 4 * 3 * SG_SLOT;

template <int W>
DI void sg_spill(const f32x16 (&acc)[5], f32x4* red, int lane, f32x4 (&res)[5]) {
#pragma unroll
  for (int G = 0; G < 20; ++G) {
    f32x4 v = {acc[G >> 2][4 * (G & 3)], acc[G >> 2][4 * (G & 3) + 1], acc[G >> 2][4 * (G & 3) + 2], acc[G >> 2][4 * (G & 3) + 3]};
    const int r = G / 5;
    if (r != W) { const int sw = W - (W > r ? 1 : 0); red[((r * 3 + sw) * 5 + (G % 5)) * 64 + lane] = v; }
    else res[G % 5] = v;
  }
}

template <int NKS>
DI void skinny_gemm(const __bf16* W, const char* actf  , int n0, unsigned lds0, char* smem, int w, int lane,
                    f32x4 (&res)[5]) {
  unsigned wv[5];
#pragma unroll
  for (int i = 0; i < 5; ++i) { const int byte = 1024 * i + 16 * lane; wv[i] = (byte / 320) * (D_ * 2) + (byte % 320); }
  const unsigned av = lane * 16;
  const char* wb = (const char*)(W + (size_t)(16 * NKS * w) * D_ + n0);
  const char* ab = actf + (size_t)(NKS * w) * 1024;
  const unsigned lw = lds0 + w * (3 * SG_SLOT);
  auto issue = [&](int j, int slot) {
    const char* wj = wb + (size_t)j * (16 * D_ * 2);
#pragma unroll
    for (int i = 0; i < 5; ++i) glds16(wj, wv[i], lw + slot * SG_SLOT + i * 1024);
    glds16(ab + j * 1024, av, lw + slot * SG_SLOT + 5120);
  };
  issue(0, 0); issue(1, 1);
  const int h = lane >> 5, blk = (lane >> 4) & 1, q = (lane & 15) >> 2, p = lane & 3;
  const unsigned ra = lw + (4 * h + q) * 320 + blk * 32 + p * 8, rb = lw + 5120 + lane * 16;
  f32x16 acc[5];
#pragma unroll
  for (int t = 0; t < 5; ++t)
#pragma unroll
    for (int i = 0; i < 16; ++i) acc[t][i] = 0.f;
  int slot = 0;
  for (int j = 0; j < NKS; ++j) {
    if (j + 1 < NKS) WAITV(6); else WAITV(0);
    if (j + 2 < NKS) { int s2 = slot + 2; if (s2 >= 3) s2 -= 3; issue(j + 2, s2); }
    const unsigned so = slot * SG_SLOT;
    u32x4 B = ldsread16(rb + so);
#pragma unroll
    for (int t = 0; t < 5; ++t) {
      u32x2 p0 = trread(ra + so + t * 64), p1 = trread(ra + so + t * 64 + 8 * 320);
      acc[t] = mfma32(u32x4{p0[0], p0[1], p1[0], p1[1]}, B, acc[t]);
    }
    asm volatile("s_waitcnt lgkmcnt(0)" ::: "memory");
    if (++slot == 3) slot = 0;
  }
  BARRIER();
  f32x4* red = (f32x4*)smem;
  if (w == 0) sg_spill<0>(acc, red, lane, res);
  else if (w == 1) sg_spill<1>(acc, red, lane, res);
  else if (w == 2) sg_spill<2>(acc, red, lane, res);
  else sg_spill<3>(acc, red, lane, res);
  BARRIER();
#pragma unroll
  for (int g = 0; g < 5; ++g)
#pragma unroll
    for (int sw = 0; sw < 3; ++sw) res[g] += red[((w * 3 + sw) * 5 + g) * 64 + lane];
}

#define EXP4(b) "v_exp_f32 v" #b ", v" #b "\n"
#define STR(x) #x
#define VEXP(n) "v_exp_f32 v[" STR(n) "], v[" STR(n) "]\n\t"
#define E4(b) VEXP(b) VEXP(b + 1) VEXP(b + 2) VEXP(b + 3)
#define QA0 "v[64:67]"
#define QA1 "v[68:71]"
#define QB0 "v[72:75]"
#define QB1 "v[76:79]"
#define OA "v[80:95]"
#define OB "v[96:111]"
#define KF0 "v[176:179]"
#define KF1 "v[180:183]"
#define VF0 "v[184:187]"
#define VF1 "v[188:191]"
#define VIDX "v192"
#define LCUR "v193"
#define LNXT "v194"
#define GOFF "v195"
#define SR(b) "v[" STR(b) ":" STR(b + 15) "]"
#define SLO(b) "v[" STR(b) ":" STR(b + 7) "]"
#define SHI(b) "v[" STR(b + 8) ":" STR(b + 15) "]"
#define PER_A(Sc, Sn, Sp, KA, KO, VO)                                              \
  "v_mfma_f32_32x32x16_bf16 " SR(Sn) ", " KF0 ", " QB0 ", 0\n\t"                    \
  "ds_read_b128 " KF0 ", " KA " offset:" STR(KO) "\n\t"                             \
  E4(Sc)                                                                            \
  "v_mfma_f32_32x32x16_bf16 " SR(Sn) ", " KF1 ", " QB1 ", " SR(Sn) "\n\t"           \
  "ds_read_b128 " KF1 ", " KA " offset:" STR(KO + 1024) "\n\t"                      \
  E4(Sc + 4)                                                                        \
  "v_smfmac_f32_32x32x32_bf16 " OB ", " VF1 ", " SHI(Sp) ", " VIDX "\n\t"           \
  "ds_read_b128 " VF1 ", " LCUR " offset:" STR(VO + 3072) "\n\t"                    \
  E4(Sc + 8)                                                                        \
  "s_waitcnt lgkmcnt(3)\n\t"                                                        \
  "v_smfmac_f32_32x32x32_bf16 " OA ", " VF0 ", " SLO(Sc) ", " VIDX "\n\t"           \
  E4(Sc + 12)
#define PER_B(Sc, Sn, Sp, KA, KO)                                                   \
  "s_waitcnt lgkmcnt(2)\n\t"                                                        \
  "v_mfma_f32_32x32x16_bf16 " SR(Sn) ", " KF0 ", " QA0 ", 0\n\t"                    \
  E4(Sc)                                                                            \
  "s_waitcnt lgkmcnt(1)\n\t"                                                        \
  "v_mfma_f32_32x32x16_bf16 " SR(Sn) ", " KF1 ", " QA1 ", " SR(Sn) "\n\t"           \
  E4(Sc + 4)                                                                        \
  "s_waitcnt lgkmcnt(0)\n\t"                                                        \
  "v_smfmac_f32_32x32x32_bf16 " OA ", " VF1 ", " SHI(Sp) ", " VIDX "\n\t"           \
  E4(Sc + 8)                                                                        \
  "v_smfmac_f32_32x32x32_bf16 " OB ", " VF0 ", " SLO(Sc) ", " VIDX "\n\t"           \
  "ds_read_b128 " VF0 ", " KA " offset:" STR(KO + 2048) "\n\t"                      \
  E4(Sc + 12)

constexpr int AT_LDS = 65536;

DI void attn_fast(const char* kv, unsigned lds0, int w, int lane, u32x4 qa0, u32x4 qa1, u32x4 qb0, u32x4 qb1, f32x16& oa, f32x16& ob) {
  const char* gnext = kv + 3 * 16384 + w * 4096;
  unsigned lcur = lds0 + lane * 16, goff = lane * 16;
  unsigned mbase = lds0 + w * 4096;
  unsigned cnt, mtmp;
  asm volatile(
      "s_waitcnt vmcnt(8)\n\t"
      "s_barrier\n\t"
      "ds_read_b128 " KF0 ", " LCUR "\n\t"
      "ds_read_b128 " KF1 ", " LCUR " offset:1024\n\t"
      "ds_read_b128 " VF0 ", " LCUR " offset:2048\n\t"
      "v_mov_b32 " VIDX ", 0xdddd\n\t"
      "v_mov_b32 v196, " GOFF "\n\t"
      "v_mov_b32 v188, 0\n\tv_mov_b32 v189, 0\n\tv_mov_b32 v190, 0\n\tv_mov_b32 v191, 0\n\t"
      "v_mov_b32 v168, 0\n\tv_mov_b32 v169, 0\n\tv_mov_b32 v170, 0\n\tv_mov_b32 v171, 0\n\t"
      "v_mov_b32 v172, 0\n\tv_mov_b32 v173, 0\n\tv_mov_b32 v174, 0\n\tv_mov_b32 v175, 0\n\t"
      "s_mov_b32 %[cnt], 0\n\t"
      "s_waitcnt lgkmcnt(0)\n\t"
      "v_mfma_f32_32x32x16_bf16 " SR(112) ", " KF0 ", " QA0 ", 0\n\t"
      "v_mfma_f32_32x32x16_bf16 " SR(112) ", " KF1 ", " QA1 ", " SR(112) "\n\t"
      "s_nop 7\n\ts_nop 7\n\t"
      "1:\n\t"
      "s_add_u32 %[mt], %[cnt], 1\n\t"
      "s_and_b32 %[mt], %[mt], 3\n\t"
      "s_lshl_b32 %[mt], %[mt], 14\n\t"
      "s_add_u32 %[mt], %[mt], %[l0]\n\t"
      "v_add_u32 " LNXT ", %[mt], " GOFF "\n\t"
      PER_A(112, 128, 160, LCUR, 4096, 0)
      PER_B(128, 144, 112, LCUR, 4096)
      PER_A(144, 160, 128, LCUR, 8192, 4096)
      PER_B(160, 112, 144, LCUR, 8192)
      PER_A(112, 128, 160, LCUR, 12288, 8192)
      PER_B(128, 144, 112, LCUR, 12288)
      "s_cmp_gt_u32 %[cnt], 12\n\t"
      "s_cbranch_scc1 2f\n\t"
      "s_add_u32 %[mt], %[cnt], 3\n\t"
      "s_and_b32 %[mt], %[mt], 3\n\t"
      "s_lshl_b32 %[mt], %[mt], 14\n\t"
      "s_add_u32 m0, %[mt], %[mb]\n\t"
      "s_waitcnt vmcnt(4)\n\t"
      "s_barrier\n\t"
      "global_load_lds_dwordx4 v196, %[gn]\n\t"
      "global_load_lds_dwordx4 v196, %[gn] offset:1024\n\t"
      "global_load_lds_dwordx4 v196, %[gn] offset:2048\n\t"
      "global_load_lds_dwordx4 v196, %[gn] offset:3072\n\t"
      "v_add_u32 v196, 0x4000, v196\n\t"
      "s_branch 3f\n\t"
      "2:\n\t"
      "s_waitcnt vmcnt(0)\n\t"
      "s_barrier\n\t"
      "3:\n\t"
      PER_A(144, 160, 128, LNXT, 0, 12288)
      PER_B(160, 112, 144, LNXT, 0)
      "v_mov_b32 " LCUR ", " LNXT "\n\t"
      "s_add_u32 %[cnt], %[cnt], 1\n\t"
      "s_cmp_lt_u32 %[cnt], 16\n\t"
      "s_cbranch_scc1 1b\n\t"
      "s_nop 1\n\t"
      "v_smfmac_f32_32x32x32_bf16 " OB ", " VF1 ", " SHI(160) ", " VIDX "\n\t"
      "s_waitcnt lgkmcnt(0)\n\t"
      "s_nop 7\n\ts_nop 7\n\t"
      : "+{v[80:95]}"(oa), "+{v[96:111]}"(ob), "+{v193}"(lcur), [cnt] "=&s"(cnt), [mt] "=&s"(mtmp)
      : "{v[64:67]}"(qa0), "{v[68:71]}"(qa1), "{v[72:75]}"(qb0), "{v[76:79]}"(qb1), "{v195}"(goff), [gn] "s"(gnext), [mb] "s"(mbase), [l0] "s"(lds0)
      : "memory", "scc",
        "v112", "v113", "v114", "v115", "v116", "v117", "v118", "v119", "v120", "v121", "v122", "v123", "v124", "v125", "v126", "v127",
        "v128", "v129", "v130", "v131", "v132", "v133", "v134", "v135", "v136", "v137", "v138", "v139", "v140", "v141", "v142", "v143",
        "v144", "v145", "v146", "v147", "v148", "v149", "v150", "v151", "v152", "v153", "v154", "v155", "v156", "v157", "v158", "v159",
        "v160", "v161", "v162", "v163", "v164", "v165", "v166", "v167", "v168", "v169", "v170", "v171", "v172", "v173", "v174", "v175",
        "v176", "v177", "v178", "v179", "v180", "v181", "v182", "v183", "v184", "v185", "v186", "v187", "v188", "v189", "v190", "v191",
        "v192", "v194", "v196");
}

DI void attn_slow(const char* kv, int lane, u32x4 q0, u32x4 q1, f32x16& o) {
  f32x16 z;
#pragma unroll
  for (int i = 0; i < 16; ++i) z[i] = 0.f;
  float m = -__builtin_inff();
  for (int kt = 0; kt < 64; ++kt) {
    const u32x4* p = (const u32x4*)(kv + (size_t)kt * 4096) + lane;
    f32x16 s = mfma32(p[0], q0, z);
    s = mfma32(p[64], q1, s);
#pragma unroll
    for (int i = 0; i < 16; ++i) m = fmaxf(m, s[i]);
  }
  m = xhalf_max(m);
  f32x16 c;
#pragma unroll
  for (int i = 0; i < 16; ++i) c[i] = -m;
  for (int kt = 0; kt < 64; ++kt) {
    const u32x4* p = (const u32x4*)(kv + (size_t)kt * 4096) + lane;
    f32x16 s = mfma32(p[0], q0, z);
    s = mfma32(p[64], q1, s);
    f32x8 lo, hi;
#pragma unroll
    for (int i = 0; i < 8; ++i) { lo[i] = __builtin_amdgcn_exp2f(s[i] + c[i]); hi[i] = __builtin_amdgcn_exp2f(s[8 + i] + c[i]); }
    o = __builtin_amdgcn_smfmac_f32_32x32x32_bf16(__builtin_bit_cast(bf16x8, p[128]), __builtin_bit_cast(bf16x16, lo), o, 0xdddd, 0, 0);
    o = __builtin_amdgcn_smfmac_f32_32x32x32_bf16(__builtin_bit_cast(bf16x8, p[192]), __builtin_bit_cast(bf16x16, hi), o, 0xdddd, 0, 0);
  }
}

DI void attn_store(char* ws, int tt, int head, int lane, const f32x16& o) {
  const int h = lane >> 5, m = lane & 31;
  const float l = h ? o[8] : o[12];
  const float rl = 1.f / l;
#pragma unroll
  for (int g = 0; g < 3; ++g) {
    if (g == 2 && h) break;
    const int cgl = 5 * head + 2 * g + h;
    char* dst = ws + WS_O + ((size_t)(tt * 40 + (cgl >> 2)) * 64 + (cgl & 1) * 32 + m) * 16 + ((cgl >> 1) & 1) * 8;
    *(u32x2*)dst = u32x2{cvtpk(o[4 * g] * rl, o[4 * g + 1] * rl), cvtpk(o[4 * g + 2] * rl, o[4 * g + 3] * rl)};
  }
}

__global__ void __launch_bounds__(256, 2)
k2_attn(const __bf16* __restrict__ wd, char* __restrict__ ws) {
  extern __shared__ __attribute__((aligned(128))) char smem[];
  const int tid = threadIdx.x, lane = tid & 63, w = rfl(tid >> 6);
  const unsigned lds0 = (unsigned)(uintptr_t)(LAS char*)smem;
  const int bid = blockIdx.x;
  if (bid < 256) {
    const int tg = (bid & 7) * 4 + ((bid >> 3) & 3), hg = bid >> 5;
    const int head = 4 * hg + w, h = lane >> 5, m = lane & 31;
    const char* kv = ws + WS_KV;
#pragma unroll
    for (int c = 0; c < 3; ++c)
#pragma unroll
      for (int i = 0; i < 4; ++i) glds16(kv + c * 16384 + w * 4096 + i * 1024, lane * 16, lds0 + c * 16384 + w * 4096 + i * 1024);
    u32x4 qf[2][2];
    float qn = 0.f;
#pragma unroll
    for (int b = 0; b < 2; ++b) {
      const char* qp = ws + WS_Q + ((size_t)(64 * tg + 32 * b + m) * D_ + 20 * head) * 2;
      u32x2 t0 = *(const u32x2*)(qp + 8 * h), t1 = *(const u32x2*)(qp + 16 + 8 * h), t2 = *(const u32x2*)(qp + 32);
      if (h) t2 = u32x2{0u, 0u};
      qf[b][0] = u32x4{t0[0], t0[1], t1[0], t1[1]};
      qf[b][1] = u32x4{t2[0], t2[1], 0u, 0u};
      float n2 = 0.f;
#pragma unroll
      for (int i = 0; i < 4; ++i) { n2 = fmaf(bflo(qf[b][0][i]), bflo(qf[b][0][i]), n2); n2 = fmaf(bfhi(qf[b][0][i]), bfhi(qf[b][0][i]), n2); }
#pragma unroll
      for (int i = 0; i < 2; ++i) { n2 = fmaf(bflo(t2[i]), bflo(t2[i]), n2); n2 = fmaf(bfhi(t2[i]), bfhi(t2[i]), n2); }
      n2 = xhalf_add(n2);
      qn = (b == 0 || !(n2 <= qn)) ? n2 : qn;
    }
    const float kn = wave_max(((const float*)(ws + WS_KN))[lane]);
    const bool unsafe = !(qn * kn <= 3600.f);
    int* flg = (int*)(smem + AT_LDS);
    const int wun = __any(unsafe) ? 1 : 0;
    if (lane == 0) flg[w] = wun;
    BARRIER();
    const int slow = rfl(flg[0] | flg[1] | flg[2] | flg[3]);
    f32x16 oa, ob;
#pragma unroll
    for (int i = 0; i < 16; ++i) { oa[i] = 0.f; ob[i] = 0.f; }
    if (!slow) {
      attn_fast(kv, lds0, w, lane, qf[0][0], qf[0][1], qf[1][0], qf[1][1], oa, ob);
    } else {
      WAITV(0);
      attn_slow(kv, lane, qf[0][0], qf[0][1], oa);
      attn_slow(kv, lane, qf[1][0], qf[1][1], ob);
    }
    attn_store(ws, 2 * tg, head, lane, oa);
    attn_store(ws, 2 * tg + 1, head, lane, ob);
  } else {
    const int g = bid - 256;
    const int tt = (g & 7) * 8 + ((g >> 3) & 7), nq = g >> 6;
    f32x4 res[5];
    skinny_gemm<27>(wd, ws + WS_H + (size_t)tt * 108 * 1024, 160 * nq, lds0, smem, w, lane, res);
    char* dst = ws + WS_HS + ((size_t)(g * 4 + w) * 5 * 64 + lane) * 8;
#pragma unroll
    for (int i = 0; i < 5; ++i) {
      float v[4];
#pragma unroll
      for (int j = 0; j < 4; ++j) v[j] = res[i][j] * __builtin_amdgcn_rcpf(1.f + __builtin_amdgcn_exp2f(-LOG2E * res[i][j]));
      *(u32x2*)(dst + i * 512) = u32x2{cvtpk(v[0], v[1]), cvtpk(v[2], v[3])};
    }
  }
}

__global__ void __launch_bounds__(256, 2)
k3_out(const __bf16* __restrict__ x, const __bf16* __restrict__ wo, char* __restrict__ ws, __bf16* __restrict__ out) {
  extern __shared__ __attribute__((aligned(128))) char smem[];
  const int tid = threadIdx.x, lane = tid & 63, w = rfl(tid >> 6);
  const unsigned lds0 = (unsigned)(uintptr_t)(LAS char*)smem;
  const int g = blockIdx.x;
  const int tt = (g & 7) * 8 + ((g >> 3) & 7), nq = g >> 6;
  f32x4 res[5];
  skinny_gemm<10>(wo, ws + WS_O + (size_t)tt * 40 * 1024, 160 * nq, lds0, smem, w, lane, res);
  const char* hs = ws + WS_HS + ((size_t)(g * 4 + w) * 5 * 64 + lane) * 8;
  const size_t o = ((size_t)(32 * tt + (lane & 31)) * D_ + 160 * nq + 40 * w + 4 * (lane >> 5));
#pragma unroll
  for (int i = 0; i < 5; ++i) {
    const u32x2 hv = *(const u32x2*)(hs + i * 512);
    const u32x2 xv = *(const u32x2*)(x + o + 8 * i);
    *(u32x2*)(out + o + 8 * i) = u32x2{cvtpk(fmaf(res[i][0], bflo(hv[0]), bflo(xv[0])), fmaf(res[i][1], bfhi(hv[0]), bfhi(xv[0]))),
                                       cvtpk(fmaf(res[i][2], bflo(hv[1]), bflo(xv[1])), fmaf(res[i][3], bfhi(hv[1]), bfhi(xv[1])))};
  }
}

extern "C" void kernel_launch(void* const* d_in, const int* in_sizes, int n_in, void* d_out, int out_size, void* d_ws, size_t ws_size,
                              hipStream_t stream) {
  const __bf16* x = (const __bf16*)d_in[0];
  const __bf16* wq = (const __bf16*)d_in[1];
  const __bf16* wk = (const __bf16*)d_in[2];
  const __bf16* wv = (const __bf16*)d_in[3];
  const __bf16* wo = (const __bf16*)d_in[4];
  const __bf16* wg = (const __bf16*)d_in[6];
  const __bf16* wu = (const __bf16*)d_in[7];
  const __bf16* wd = (const __bf16*)d_in[8];
  char* ws = (char*)d_ws;
  (void)hipFuncSetAttribute((const void*)k1_proj, hipFuncAttributeMaxDynamicSharedMemorySize, K1_LDS);
  (void)hipFuncSetAttribute((const void*)k2_attn, hipFuncAttributeMaxDynamicSharedMemorySize, SG_LDS);
  (void)hipFuncSetAttribute((const void*)k3_out, hipFuncAttributeMaxDynamicSharedMemorySize, SG_LDS);
  k1_proj<<<256, 512, K1_LDS, stream>>>(x, wq, wk, wv, wg, wu, ws);
  k2_attn<<<512, 256, SG_LDS, stream>>>(wd, ws);
  k3_out<<<256, 256, SG_LDS, stream>>>(x, wo, ws, (__bf16*)d_out);
}
```

```cpp
#include <hip/hip_runtime.h>
#include <hip/hip_bf16.h>
#include <stdint.h>

typedef __bf16 bf16x8 __attribute__((ext_vector_type(8)));
typedef __bf16 bf16x16 __attribute__((ext_vector_type(16)));
typedef __bf16 bf16x2 __attribute__((ext_vector_type(2)));
typedef short s16x4 __attribute__((ext_vector_type(4)));
typedef float f32x16 __attribute__((ext_vector_type(16)));
typedef float f32x8 __attribute__((ext_vector_type(8)));
typedef float f32x4 __attribute__((ext_vector_type(4)));
typedef unsigned u32x4 __attribute__((ext_vector_type(4)));
typedef unsigned u32x2 __attribute__((ext_vector_type(2)));
typedef unsigned long long u64;
#define LAS __attribute__((address_space(3)))
#define GAS __attribute__((address_space(1)))
#define DI __device__ __forceinline__
#define STR_(x) #x
#define STR(x) STR_(x)

constexpr int S_ = 2048, D_ = 640, F_ = 1728;
constexpr float LOG2E = 1.4426950408889634f;
constexpr float QSCALE = 0.22360679774997896f * LOG2E;
constexpr int LDS_BYTES = 81920;
#define ASLEEP 16
#define PSLEEP 2
#define GPRIO 0
#define APRIO 1

constexpr unsigned WS_E = 0;
constexpr unsigned FS = 128;
constexpr unsigned WS_KN = 4096;
constexpr unsigned WS_XN = 16384;
constexpr unsigned WS_KV = WS_XN + 128 * 20 * 1024;
constexpr unsigned WS_H = WS_KV + 64 * 4096;
constexpr unsigned WS_O = WS_H + 128 * 54 * 1024;
constexpr unsigned WS_WD = WS_O + 128 * 20 * 1024;
constexpr unsigned WS_F0 = WS_WD + 8 * 54 * 5 * 1024;
constexpr unsigned WS_F1 = WS_F0 + 256 * FS;
constexpr unsigned WS_F2 = WS_F1 + 256 * FS;
constexpr unsigned WS_F3 = WS_F2 + 256 * FS;
constexpr unsigned WS_END = WS_F3 + 256 * FS;

DI unsigned cvtpk(float lo, float hi) { bf16x2 v = {(__bf16)lo, (__bf16)hi}; return __builtin_bit_cast(unsigned, v); }
DI float bflo(unsigned u) { return __uint_as_float(u << 16); }
DI float bfhi(unsigned u) { return __uint_as_float(u & 0xffff0000u); }
DI f32x16 mfma32(u32x4 a, u32x4 b, f32x16 c) {
  return __builtin_amdgcn_mfma_f32_32x32x16_bf16(__builtin_bit_cast(bf16x8, a), __builtin_bit_cast(bf16x8, b), c, 0, 0, 0);
}
DI f32x4 mfma16(u32x4 a, u32x4 b, f32x4 c) {
  return __builtin_amdgcn_mfma_f32_16x16x32_bf16(__builtin_bit_cast(bf16x8, a), __builtin_bit_cast(bf16x8, b), c, 0, 0, 0);
}
DI u32x2 trread(unsigned addr) { return __builtin_bit_cast(u32x2, __builtin_amdgcn_ds_read_tr16_b64_v4i16((LAS s16x4*)(uintptr_t)addr)); }
DI u32x4 trfrag(unsigned addr, unsigned d) { u32x2 a = trread(addr), b = trread(addr + d); return u32x4{a[0], a[1], b[0], b[1]}; }
template <typename T> DI T ldsr(unsigned addr) { return *(LAS T*)(uintptr_t)addr; }
template <typename T> DI void ldsw(unsigned addr, T v) { *(LAS T*)(uintptr_t)addr = v; }
DI void glds16(const void* sbase, unsigned voff, unsigned lds) {
  asm volatile("s_mov_b32 m0, %2\n\ts_nop 0\n\tglobal_load_lds_dwordx4 %0, %1" ::"v"(voff), "s"(sbase), "s"(lds) : "memory");
}
DI void glds16_sc1(const void* sbase, unsigned voff, unsigned lds) {
  asm volatile("s_mov_b32 m0, %2\n\ts_nop 0\n\tglobal_load_lds_dwordx4 %0, %1 sc1" ::"v"(voff), "s"(sbase), "s"(lds) : "memory");
}
template <int CTRL> DI float dppf(float v) { return __int_as_float(__builtin_amdgcn_update_dpp(0, __float_as_int(v), CTRL, 0xf, 0xf, true)); }
DI float swap16(float v, bool& dummy) { return v; }
DI float sum16(float v) {
  v += dppf<0xB1>(v); v += dppf<0x4E>(v); v += dppf<0x141>(v); v += dppf<0x140>(v); return v;
}
DI float max16(float v) {
  v = fmaxf(v, dppf<0xB1>(v)); v = fmaxf(v, dppf<0x4E>(v)); v = fmaxf(v, dppf<0x141>(v)); v = fmaxf(v, dppf<0x140>(v)); return v;
}
DI float x16_add(float v) { auto r = __builtin_amdgcn_permlane16_swap(__float_as_uint(v), __float_as_uint(v), false, false); return __uint_as_float(r[0]) + __uint_as_float(r[1]); }
DI float x16_max(float v) { auto r = __builtin_amdgcn_permlane16_swap(__float_as_uint(v), __float_as_uint(v), false, false); return fmaxf(__uint_as_float(r[0]), __uint_as_float(r[1])); }
DI float x32_add(float v) { auto r = __builtin_amdgcn_permlane32_swap(__float_as_uint(v), __float_as_uint(v), false, false); return __uint_as_float(r[0]) + __uint_as_float(r[1]); }
DI float x32_max(float v) { auto r = __builtin_amdgcn_permlane32_swap(__float_as_uint(v), __float_as_uint(v), false, false); return fmaxf(__uint_as_float(r[0]), __uint_as_float(r[1])); }
DI float wave_max(float v) { return x32_max(x16_max(max16(v))); }
DI int rfl(int v) { return __builtin_amdgcn_readfirstlane(v); }
#define WAITV(n) asm volatile("s_waitcnt vmcnt(" #n ")" ::: "memory")
#define WAITL() asm volatile("s_waitcnt lgkmcnt(0)" ::: "memory")
#define BARRIER() asm volatile("s_waitcnt lgkmcnt(0)\n\ts_barrier" ::: "memory")
#define RLX_AGENT __ATOMIC_RELAXED, __HIP_MEMORY_SCOPE_AGENT
typedef __amdgpu_buffer_rsrc_t rsrc_t;
DI u32x4 bld16(rsrc_t r, unsigned voff, unsigned soff) { return __builtin_amdgcn_raw_buffer_load_b128(r, voff, soff, 16); }
DI void bst16(rsrc_t r, u32x4 v, unsigned voff, unsigned soff) { __builtin_amdgcn_raw_buffer_store_b128(v, r, voff + soff, 0, 16); }
DI void bst8(rsrc_t r, u32x2 v, unsigned voff, unsigned soff) { __builtin_amdgcn_raw_buffer_store_b64(v, r, voff, soff, 16); }
DI void bst4(rsrc_t r, unsigned v, unsigned voff, unsigned soff) { __builtin_amdgcn_raw_buffer_store_b32(v, r, voff, soff, 16); }

#define STAMP(i)
#define STAMPC(i)

DI u64 mktag(u64 ep) {
  const u64 e = ((u64)(unsigned)rfl((int)(ep >> 32)) << 32) | (unsigned)rfl((int)ep);
  return (e ^ 0x9E3779B97F4A7C15ull) * 0xD1342543DE82EF95ull + 1;
}
DI void poll(const char* ws, unsigned off, u64 tag) {
  const GAS u64* p = (const GAS u64*)(ws + off);
  for (int spins = 0; spins < (1 << 16); ++spins) {
    const u64 v = __hip_atomic_load(p, RLX_AGENT);
    if (__all(v == tag)) break;
    __builtin_amdgcn_s_sleep(PSLEEP);
  }
  asm volatile("" ::: "memory");
}
DI void publish(char* ws, unsigned off, u64 tag) {
  asm volatile("s_waitcnt vmcnt(0) lgkmcnt(0)\n\ts_barrier" ::: "memory");
  if (threadIdx.x == 0) __hip_atomic_store((GAS u64*)(ws + off), tag, RLX_AGENT);
}

struct T80 { u32x4 a0[5]; unsigned ra; };
DI void t80_prefetch(T80& t, const __bf16* W  , unsigned lds0, int w, int lane) {
  unsigned wv[5];
#pragma unroll
  for (int i = 0; i < 5; ++i) { const int byte = 1024 * i + 16 * lane; wv[i] = (byte / 160) * (D_ * 2) + (byte % 160); }
  const char* wb = (const char*)(W + (size_t)(160 * w) * D_);
  const unsigned lw = lds0 + w * 20480;
#pragma unroll
  for (int s = 0; s < 4; ++s)
#pragma unroll
    for (int i = 0; i < 5; ++i) glds16(wb + s * (32 * D_ * 2), wv[i], lw + s * 5120 + i * 1024);
  t.ra = lw + (4 * (lane >> 4) + ((lane & 15) >> 2)) * 160 + (lane & 3) * 8;
  WAITV(15);
#pragma unroll
  for (int n = 0; n < 5; ++n) t.a0[n] = trfrag(t.ra + n * 32, 16 * 160);
  asm volatile("s_waitcnt lgkmcnt(0)" : "+v"(t.a0[0]), "+v"(t.a0[1]), "+v"(t.a0[2]), "+v"(t.a0[3]), "+v"(t.a0[4])::"memory");
#pragma unroll
  for (int i = 0; i < 5; ++i) glds16(wb + 4 * (32 * D_ * 2), wv[i], lw + i * 1024);
}
DI void red4(const f32x4 (&acc)[5][4], unsigned lds0, int w, int lane, f32x4 (&res)[5]) {
  const unsigned base = lds0 + lane * 16;
  BARRIER();
#pragma unroll
  for (int tb = 1; tb < 4; ++tb) {
    const unsigned d = base + (((tb + w) & 3) * 3 + tb - 1) * 5120;
#pragma unroll
    for (int n = 0; n < 5; ++n) ldsw<f32x4>(d + n * 1024, acc[n][tb]);
  }
  BARRIER();
#pragma unroll
  for (int n = 0; n < 5; ++n) {
    res[n] = acc[n][0];
#pragma unroll
    for (int sw = 0; sw < 3; ++sw) res[n] += ldsr<f32x4>(base + w * 15360 + (sw * 5 + n) * 1024);
  }
}
DI void t80_compute(const T80& t, rsrc_t rs, unsigned act  , unsigned lds0, int w, int lane, f32x4 (&res)[5]) {
  u32x4 b[5][4];
  const unsigned so = act + w * 5120;
#pragma unroll
  for (int s = 0; s < 5; ++s)
#pragma unroll
    for (int tb = 0; tb < 4; ++tb) b[s][tb] = bld16(rs, lane * 16 + (s & 3) * 1024, so + ((tb + w) & 3) * 20480 + (s >> 2) * 4096);
  __builtin_amdgcn_sched_barrier(0);
  f32x4 acc[5][4];
#pragma unroll
  for (int n = 0; n < 5; ++n)
#pragma unroll
    for (int tb = 0; tb < 4; ++tb) acc[n][tb] = mfma16(t.a0[n], b[0][tb], f32x4{0.f, 0.f, 0.f, 0.f});
#pragma unroll
  for (int s = 1; s < 5; ++s)
#pragma unroll
    for (int n = 0; n < 5; ++n) {
      const u32x4 a = trfrag(t.ra + (s & 3) * 5120 + n * 32, 16 * 160);
#pragma unroll
      for (int tb = 0; tb < 4; ++tb) acc[n][tb] = mfma16(a, b[s][tb], acc[n][tb]);
    }
  red4(acc, lds0, w, lane, res);
}

#define VEXP(n) "v_exp_f32 v[" STR(n) "], v[" STR(n) "]\n\t"
#define E4(b) VEXP(b) VEXP(b + 1) VEXP(b + 2) VEXP(b + 3)
#define QA0 "v[64:67]"
#define QA1 "v[68:71]"
#define QB0 "v[72:75]"
#define QB1 "v[76:79]"
#define OA "v[80:95]"
#define OB "v[96:111]"
#define KF0 "v[176:179]"
#define KF1 "v[180:183]"
#define VF0 "v[184:187]"
#define VF1 "v[188:191]"
#define VIDX "v192"
#define LCUR "v193"
#define LNXT "v194"
#define GOFF "v195"
#define SR(b) "v[" STR(b) ":" STR(b + 15) "]"
#define SLO(b) "v[" STR(b) ":" STR(b + 7) "]"
#define SHI(b) "v[" STR(b + 8) ":" STR(b + 15) "]"
#define PER_A(Sc, Sn, Sp, KA, KO, VO)                                               \
  "v_mfma_f32_32x32x16_bf16 " SR(Sn) ", " KF0 ", " QB0 ", 0\n\t"                    \
  "ds_read_b128 " KF0 ", " KA " offset:" STR(KO) "\n\t"                             \
  E4(Sc)                                                                            \
  "v_mfma_f32_32x32x16_bf16 " SR(Sn) ", " KF1 ", " QB1 ", " SR(Sn) "\n\t"           \
  "ds_read_b128 " KF1 ", " KA " offset:" STR(KO + 1024) "\n\t"                      \
  E4(Sc + 4)                                                                        \
  "v_smfmac_f32_32x32x32_bf16 " OB ", " VF1 ", " SHI(Sp) ", " VIDX "\n\t"           \
  "ds_read_b128 " VF1 ", " LCUR " offset:" STR(VO + 3072) "\n\t"                    \
  E4(Sc + 8)                                                                        \
  "s_waitcnt lgkmcnt(3)\n\t"                                                        \
  "v_smfmac_f32_32x32x32_bf16 " OA ", " VF0 ", " SLO(Sc) ", " VIDX "\n\t"           \
  E4(Sc + 12)
#define PER_B(Sc, Sn, Sp, KA, KO, X)                                                \
  "s_waitcnt lgkmcnt(1)\n\t"                                                        \
  "v_mfma_f32_32x32x16_bf16 " SR(Sn) ", " KF0 ", " QA0 ", 0\n\t"                    \
  X                                                                                 \
  E4(Sc)                                                                            \
  "v_mfma_f32_32x32x16_bf16 " SR(Sn) ", " KF1 ", " QA1 ", " SR(Sn) "\n\t"           \
  E4(Sc + 4)                                                                        \
  "s_waitcnt lgkmcnt(0)\n\t"                                                        \
  "v_smfmac_f32_32x32x32_bf16 " OA ", " VF1 ", " SHI(Sp) ", " VIDX "\n\t"           \
  E4(Sc + 8)                                                                        \
  "v_smfmac_f32_32x32x32_bf16 " OB ", " VF0 ", " SLO(Sc) ", " VIDX "\n\t"           \
  "ds_read_b128 " VF0 ", " KA " offset:" STR(KO + 2048) "\n\t"                      \
  E4(Sc + 12)
#define GL(o) "global_load_lds_dwordx4 v196, %[gn] offset:" STR(o) " sc1\n\t"

DI void attn_fast(const char* kv, unsigned lds0, int w, int lane, u32x4 qa0, u32x4 qa1, u32x4 qb0, u32x4 qb1, f32x16& oa, f32x16& ob) {
  const char* gnext = kv + w * 4096;
  unsigned lcur = lds0 + lane * 16, goff = lane * 16;
  unsigned mbase = lds0 + w * 4096;
  unsigned cnt, mtmp;
  asm volatile(
      "ds_read_b128 " KF0 ", " LCUR "\n\t"
      "ds_read_b128 " KF1 ", " LCUR " offset:1024\n\t"
      "ds_read_b128 " VF0 ", " LCUR " offset:2048\n\t"
      "v_mov_b32 " VIDX ", 0xdddd\n\t"
      "v_mov_b32 v188, 0\n\tv_mov_b32 v189, 0\n\tv_mov_b32 v190, 0\n\tv_mov_b32 v191, 0\n\t"
      "v_mov_b32 v168, 0\n\tv_mov_b32 v169, 0\n\tv_mov_b32 v170, 0\n\tv_mov_b32 v171, 0\n\t"
      "v_mov_b32 v172, 0\n\tv_mov_b32 v173, 0\n\tv_mov_b32 v174, 0\n\tv_mov_b32 v175, 0\n\t"
      "s_mov_b32 %[cnt], 0\n\t"
      "s_waitcnt lgkmcnt(0)\n\t"
      "v_mfma_f32_32x32x16_bf16 " SR(112) ", " KF0 ", " QA0 ", 0\n\t"
      "v_mfma_f32_32x32x16_bf16 " SR(112) ", " KF1 ", " QA1 ", " SR(112) "\n\t"
      "s_nop 7\n\ts_nop 7\n\t"
      "1:\n\t"
      "s_add_u32 %[mt], %[cnt], 1\n\t"
      "s_and_b32 %[mt], %[mt], 3\n\t"
      "s_lshl_b32 %[mt], %[mt], 14\n\t"
      "s_add_u32 %[mt], %[mt], %[l0]\n\t"
      "v_add_u32 " LNXT ", %[mt], " GOFF "\n\t"
      "s_add_u32 %[mt], %[cnt], 2\n\t"
      "s_min_u32 %[mt], %[mt], 15\n\t"
      "s_lshl_b32 %[mt], %[mt], 14\n\t"
      "v_add_u32 v196, %[mt], " GOFF "\n\t"
      "s_add_u32 %[mt], %[cnt], 2\n\t"
      "s_and_b32 %[mt], %[mt], 3\n\t"
      "s_lshl_b32 %[mt], %[mt], 14\n\t"
      "s_add_u32 m0, %[mt], %[mb]\n\t"
      PER_A(112, 128, 160, LCUR, 4096, 0)
      PER_B(128, 144, 112, LCUR, 4096, GL(0))
      PER_A(144, 160, 128, LCUR, 8192, 4096)
      PER_B(160, 112, 144, LCUR, 8192, GL(1024))
      PER_A(112, 128, 160, LCUR, 12288, 8192)
      PER_B(128, 144, 112, LCUR, 12288, GL(2048))
      "s_waitcnt vmcnt(3)\n\t"
      "s_barrier\n\t"
      PER_A(144, 160, 128, LNXT, 0, 12288)
      PER_B(160, 112, 144, LNXT, 0, GL(3072))
      "v_mov_b32 " LCUR ", " LNXT "\n\t"
      "s_add_u32 %[cnt], %[cnt], 1\n\t"
      "s_cmp_lt_u32 %[cnt], 16\n\t"
      "s_cbranch_scc1 1b\n\t"
      "s_nop 1\n\t"
      "v_smfmac_f32_32x32x32_bf16 " OB ", " VF1 ", " SHI(160) ", " VIDX "\n\t"
      "s_waitcnt lgkmcnt(0)\n\t"
      "s_nop 7\n\ts_nop 7\n\t"
      : "+{v[80:95]}"(oa), "+{v[96:111]}"(ob), "+{v193}"(lcur), [cnt] "=&s"(cnt), [mt] "=&s"(mtmp)
      : "{v[64:67]}"(qa0), "{v[68:71]}"(qa1), "{v[72:75]}"(qb0), "{v[76:79]}"(qb1), "{v195}"(goff), [gn] "s"(gnext), [mb] "s"(mbase), [l0] "s"(lds0)
      : "memory", "scc",
        "v112", "v113", "v114", "v115", "v116", "v117", "v118", "v119", "v120", "v121", "v122", "v123", "v124", "v125", "v126", "v127",
        "v128", "v129", "v130", "v131", "v132", "v133", "v134", "v135", "v136", "v137", "v138", "v139", "v140", "v141", "v142", "v143",
        "v144", "v145", "v146", "v147", "v148", "v149", "v150", "v151", "v152", "v153", "v154", "v155", "v156", "v157", "v158", "v159",
        "v160", "v161", "v162", "v163", "v164", "v165", "v166", "v167", "v168", "v169", "v170", "v171", "v172", "v173", "v174", "v175",
        "v176", "v177", "v178", "v179", "v180", "v181", "v182", "v183", "v184", "v185", "v186", "v187", "v188", "v189", "v190", "v191",
        "v192", "v194", "v196");
}

DI void attn_slow(rsrc_t rs, int lane, u32x4 q0, u32x4 q1, f32x16& o) {
  f32x16 z;
#pragma unroll
  for (int i = 0; i < 16; ++i) z[i] = 0.f;
  float m = -__builtin_inff();
  for (int kt = 0; kt < 64; ++kt) {
    const unsigned so = WS_KV + kt * 4096;
    f32x16 s = mfma32(bld16(rs, lane * 16, so), q0, z);
    s = mfma32(bld16(rs, lane * 16 + 1024, so), q1, s);
#pragma unroll
    for (int i = 0; i < 16; ++i) m = fmaxf(m, s[i]);
  }
  m = x32_max(m);
  for (int kt = 0; kt < 64; ++kt) {
    const unsigned so = WS_KV + kt * 4096;
    f32x16 s = mfma32(bld16(rs, lane * 16, so), q0, z);
    s = mfma32(bld16(rs, lane * 16 + 1024, so), q1, s);
    f32x8 lo, hi;
#pragma unroll
    for (int i = 0; i < 8; ++i) { lo[i] = __builtin_amdgcn_exp2f(s[i] - m); hi[i] = __builtin_amdgcn_exp2f(s[8 + i] - m); }
    o = __builtin_amdgcn_smfmac_f32_32x32x32_bf16(__builtin_bit_cast(bf16x8, bld16(rs, lane * 16 + 2048, so)), __builtin_bit_cast(bf16x16, lo), o, 0xdddd, 0, 0);
    o = __builtin_amdgcn_smfmac_f32_32x32x32_bf16(__builtin_bit_cast(bf16x8, bld16(rs, lane * 16 + 3072, so)), __builtin_bit_cast(bf16x16, hi), o, 0xdddd, 0, 0);
  }
}

DI void attn_store(rsrc_t rs, int t32, int head, int lane, const f32x16& o) {
  const int h = lane >> 5, m = lane & 31;
  const float rl = 1.f / (h ? o[8] : o[12]);
  const unsigned tb = WS_O + (2 * t32 + (m >> 4)) * 20480 + (m & 15) * 16;
#pragma unroll
  for (int g = 0; g < 3; ++g) {
    const int c = 5 * head + 2 * g + h;
    const u32x2 v = {cvtpk(o[4 * g] * rl, o[4 * g + 1] * rl), cvtpk(o[4 * g + 2] * rl, o[4 * g + 3] * rl)};
    if (g < 2 || !h) bst8(rs, v, tb + (c >> 3) * 1024 + (c & 3) * 256 + ((c >> 2) & 1) * 8, 0);
  }
}

#define VR4(b) "v[" STR(b) ":" STR(b + 3) "]"
#define VR2(b) "v[" STR(b) ":" STR(b + 1) "]"
#define G_MF(n, ab, bs)                                                                                              \
  "v_mfma_f32_16x16x32_bf16 " VR4(128 + (n) * 16) ", " VR4(48 + (ab) * 4) ", " VR4(64 + (bs) * 16) ", " VR4(128 + (n) * 16) "\n\t"             \
  "v_mfma_f32_16x16x32_bf16 " VR4(132 + (n) * 16) ", " VR4(48 + (ab) * 4) ", " VR4(68 + (bs) * 16) ", " VR4(132 + (n) * 16) "\n\t"             \
  "v_mfma_f32_16x16x32_bf16 " VR4(136 + (n) * 16) ", " VR4(48 + (ab) * 4) ", " VR4(72 + (bs) * 16) ", " VR4(136 + (n) * 16) "\n\t"             \
  "v_mfma_f32_16x16x32_bf16 " VR4(140 + (n) * 16) ", " VR4(48 + (ab) * 4) ", " VR4(76 + (bs) * 16) ", " VR4(140 + (n) * 16) "\n\t"
#define G_RD(f)                                                                                                      \
  "ds_read_b64_tr_b16 " VR2(48 + ((f) & 3) * 4) ", v[44+((" STR(f) ")&3)] offset:" STR(((f) >> 2) * 4096) "\n\t"        \
  "ds_read_b64_tr_b16 " VR2(50 + ((f) & 3) * 4) ", v[44+((" STR(f) ")&3)] offset:" STR(((f) >> 2) * 4096 + 2048) "\n\t"
#define G_SLOT(f, wl, X) "s_waitcnt lgkmcnt(" STR(wl) ")\n\t" G_MF((f) & 7, (f) & 3, (f) >> 3) X
#define G_S(f, X) G_SLOT(f, 4, G_RD(f + 3) X)
#define G_BL(set, tb, base, off) "global_load_dwordx4 " VR4(64 + (set) * 16 + (tb) * 4) ", v[32+" STR(tb) "], " base " offset:" STR(off) " sc1\n\t"
#define G_WL(rb, base) "global_load_lds_dwordx4 v[36+" STR(rb) "], " base "\n\ts_add_u32 m0, m0, 0x400\n\t"
#define G_STEP(j, X0, X1, X2, X3, X4, X5, X6, X7) G_S(8 * j, X0) G_S(8 * j + 1, X1) G_S(8 * j + 2, X2) G_S(8 * j + 3, X3) G_S(8 * j + 4, X4) G_S(8 * j + 5, X5) G_S(8 * j + 6, X6) G_S(8 * j + 7, X7)
#define G_LAST(X0, X1, X2, X3) G_S(24, X0) G_S(25, X1) G_S(26, X2) G_S(27, X3) G_S(28, "") G_SLOT(29, 4, "") G_SLOT(30, 2, "") G_SLOT(31, 0, "")
#define G_HEAD G_RD(0) G_RD(1) G_RD(2)

DI void gu_loop(const char* pg, const char* pu, const char* px, unsigned m0base, u32x4 bvo, u32x4 wvo, u32x4 ra, int c0, f32x16 (&acc)[8]) {
  unsigned cnt, grp, t0;
  u64 sg, su, sx, sn;
  asm volatile(
      "s_waitcnt lgkmcnt(0)\n\t"
      "s_lshl_b32 %[t0], %[c0], 12\n\t"
      "s_mov_b64 %[sx], %[px]\n\t"
      "s_mov_b32 %[grp], %[c0]\n\t"
      "s_add_u32 s40, s40, %[t0]\n\ts_addc_u32 s41, s41, 0\n\t"
      G_BL(0, 0, "%[sx]", 0) G_BL(0, 1, "%[sx]", 0) G_BL(0, 2, "%[sx]", 0) G_BL(0, 3, "%[sx]", 0)
      G_BL(1, 0, "%[sx]", 1024) G_BL(1, 1, "%[sx]", 1024) G_BL(1, 2, "%[sx]", 1024) G_BL(1, 3, "%[sx]", 1024)
      G_BL(2, 0, "%[sx]", 2048) G_BL(2, 1, "%[sx]", 2048) G_BL(2, 2, "%[sx]", 2048) G_BL(2, 3, "%[sx]", 2048)
      "s_mov_b32 %[cnt], 0\n\t"
      "1:\n\t"
      "s_add_u32 %[grp], %[grp], 1\n\t"
      "s_cmp_eq_u32 %[grp], 5\n\t"
      "s_cselect_b32 %[grp], 0, %[grp]\n\t"
      "s_mov_b64 %[sg], %[pg]\n\ts_mov_b64 %[su], %[pu]\n\ts_mov_b64 %[sn], %[px]\n\t"
      "s_mul_i32 %[t0], %[grp], 442368\n\t"
      "s_add_u32 s36, s36, %[t0]\n\ts_addc_u32 s37, s37, 0\n\t"
      "s_add_u32 s38, s38, %[t0]\n\ts_addc_u32 s39, s39, 0\n\t"
      "s_lshl_b32 %[t0], %[grp], 12\n\t"
      "s_add_u32 s42, s42, %[t0]\n\ts_addc_u32 s43, s43, 0\n\t"
      "s_and_b32 %[t0], %[cnt], 1\n\t"
      "s_xor_b32 %[t0], %[t0], 1\n\t"
      "s_lshl_b32 %[t0], %[t0], 15\n\t"
      "s_add_u32 m0, %[t0], %[mb]\n\t"
      "s_waitcnt vmcnt(8)\n\t"
      "s_barrier\n\t"
      G_HEAD
      G_STEP(0, G_WL(0, "%[sg]"), G_WL(1, "%[sg]"), G_WL(2, "%[sg]"), G_WL(3, "%[sg]"), G_BL(3, 0, "%[sx]", 3072), G_BL(3, 1, "%[sx]", 3072), G_BL(3, 2, "%[sx]", 3072), G_BL(3, 3, "%[sx]", 3072))
      "s_waitcnt vmcnt(12)\n\t"
      G_STEP(1, G_WL(0, "%[su]"), G_WL(1, "%[su]"), G_WL(2, "%[su]"), G_WL(3, "%[su]"), G_BL(0, 0, "%[sn]", 0), G_BL(0, 1, "%[sn]", 0), G_BL(0, 2, "%[sn]", 0), G_BL(0, 3, "%[sn]", 0))
      "s_waitcnt vmcnt(16)\n\t"
      G_STEP(2, G_BL(1, 0, "%[sn]", 1024), "", G_BL(1, 1, "%[sn]", 1024), "", G_BL(1, 2, "%[sn]", 1024), "", G_BL(1, 3, "%[sn]", 1024), "")
      "s_waitcnt vmcnt(12)\n\t"
      G_LAST(G_BL(2, 0, "%[sn]", 2048), G_BL(2, 1, "%[sn]", 2048), G_BL(2, 2, "%[sn]", 2048), G_BL(2, 3, "%[sn]", 2048))
      "v_xor_b32 v44, 0x8000, v44\n\tv_xor_b32 v45, 0x8000, v45\n\tv_xor_b32 v46, 0x8000, v46\n\tv_xor_b32 v47, 0x8000, v47\n\t"
      "s_mov_b64 %[sx], %[sn]\n\t"
      "s_add_u32 %[cnt], %[cnt], 1\n\t"
      "s_cmp_lt_u32 %[cnt], 4\n\t"
      "s_cbranch_scc1 1b\n\t"
      "s_waitcnt vmcnt(8)\n\t"
      "s_barrier\n\t"
      G_HEAD
      G_STEP(0, G_BL(3, 0, "%[sx]", 3072), G_BL(3, 1, "%[sx]", 3072), G_BL(3, 2, "%[sx]", 3072), G_BL(3, 3, "%[sx]", 3072), "", "", "", "")
      "s_waitcnt vmcnt(8)\n\t"
      G_STEP(1, "", "", "", "", "", "", "", "")
      "s_waitcnt vmcnt(4)\n\t"
      G_STEP(2, "", "", "", "", "", "", "", "")
      "s_waitcnt vmcnt(0)\n\t"
      G_LAST("", "", "", "")
      "s_nop 7\n\ts_nop 3\n\t"
      : "+{v[128:143]}"(acc[0]), "+{v[144:159]}"(acc[1]), "+{v[160:175]}"(acc[2]), "+{v[176:191]}"(acc[3]),
        "+{v[192:207]}"(acc[4]), "+{v[208:223]}"(acc[5]), "+{v[224:239]}"(acc[6]), "+{v[240:255]}"(acc[7]),
        "+{v[44:47]}"(ra), [cnt] "=&s"(cnt), [grp] "=&s"(grp), [t0] "=&s"(t0),
        [sg] "=&{s[36:37]}"(sg), [su] "=&{s[38:39]}"(su), [sx] "=&{s[40:41]}"(sx), [sn] "=&{s[42:43]}"(sn)
      : "{v[32:35]}"(bvo), "{v[36:39]}"(wvo), [pg] "s"(pg), [pu] "s"(pu), [px] "s"(px), [mb] "s"(m0base), [c0] "s"(c0)
      : "memory", "scc",
        "v48", "v49", "v50", "v51", "v52", "v53", "v54", "v55", "v56", "v57", "v58", "v59", "v60", "v61", "v62", "v63",
        "v64", "v65", "v66", "v67", "v68", "v69", "v70", "v71", "v72", "v73", "v74", "v75", "v76", "v77", "v78", "v79",
        "v80", "v81", "v82", "v83", "v84", "v85", "v86", "v87", "v88", "v89", "v90", "v91", "v92", "v93", "v94", "v95",
        "v96", "v97", "v98", "v99", "v100", "v101", "v102", "v103", "v104", "v105", "v106", "v107", "v108", "v109", "v110", "v111",
        "v112", "v113", "v114", "v115", "v116", "v117", "v118", "v119", "v120", "v121", "v122", "v123", "v124", "v125", "v126", "v127");
}

DI float silu_f(float g) { return g * __builtin_amdgcn_rcpf(1.f + __builtin_amdgcn_exp2f(-LOG2E * g)); }

__global__ void __launch_bounds__(256, 2)
mega(const __bf16* __restrict__ x, const __bf16* __restrict__ wq, const __bf16* __restrict__ wk, const __bf16* __restrict__ wv,
     const __bf16* __restrict__ wo, const __bf16* __restrict__ wg, const __bf16* __restrict__ wu, const __bf16* __restrict__ wd,
     char* __restrict__ ws, __bf16* __restrict__ out) {
  extern __shared__ __attribute__((aligned(1024))) char smem[];
  const int tid = threadIdx.x, lane = tid & 63, w = rfl(tid >> 6);
  const unsigned lds0 = (unsigned)(uintptr_t)(LAS char*)smem;
  const int bid = blockIdx.x & 255, xcd = bid & 7, loc = bid >> 3;
  const int tg = 4 * xcd + (loc & 3), hc = loc >> 2;
  const rsrc_t rs = __builtin_amdgcn_make_buffer_rsrc(ws, 0, WS_END, 0x00020000);
  STAMP(0); STAMPC(30);
  const int g4 = lane >> 4, m16 = lane & 15;

  if (blockIdx.x < 256) {
    __builtin_amdgcn_s_setprio(APRIO);
    __builtin_amdgcn_s_sleep(ASLEEP);
    u64 ep;
    asm volatile("global_load_dwordx2 %0, %1, %2 sc1" : "=v"(ep) : "v"(0u), "s"(ws) : "memory");
    T80 t;
    t80_prefetch(t, wq + 80 * hc, lds0, w, lane);
    asm volatile("" : "+v"(ep));
    const u64 tag = mktag(ep);
    STAMP(1);
    poll(ws, WS_F0 + (8 * tg + (lane & 7)) * FS, tag);
    WAITV(0);
    STAMP(2);
    f32x4 res[5];
    t80_compute(t, rs, WS_XN + 4 * tg * 20480, lds0, w, lane, res);
    STAMP(3);
    const unsigned qi = lds0 + 65536;
#pragma unroll
    for (int n = 0; n < 5; ++n)
      ldsw<u32x2>(qi + (16 * w + m16) * 160 + n * 32 + g4 * 8, u32x2{cvtpk(res[n][0] * QSCALE, res[n][1] * QSCALE), cvtpk(res[n][2] * QSCALE, res[n][3] * QSCALE)});
    const int prod = 16 * (lane >> 2) + 4 * w + (lane & 3);
    poll(ws, WS_F1 + prod * FS, tag);
    STAMP(4);
    const u32x2 knv = __builtin_amdgcn_raw_buffer_load_b64(rs, WS_KN + prod * 8, 0, 16);
    BARRIER();
    const char* kv = ws + WS_KV;
#pragma unroll
    for (int c = 0; c < 2; ++c)
#pragma unroll
      for (int i = 0; i < 4; ++i) glds16_sc1(kv + c * 16384 + w * 4096 + i * 1024, lane * 16, lds0 + c * 16384 + w * 4096 + i * 1024);
    const int h = lane >> 5, m = lane & 31;
    u32x4 qf[2][2];
    float qn = 0.f;
#pragma unroll
    for (int b = 0; b < 2; ++b) {
      const unsigned qp = qi + (32 * b + m) * 160 + 40 * w;
      const u32x2 t0 = ldsr<u32x2>(qp + 8 * h), t1 = ldsr<u32x2>(qp + 16 + 8 * h);
      u32x2 t2 = ldsr<u32x2>(qp + 32);
      if (h) t2 = u32x2{0u, 0u};
      qf[b][0] = u32x4{t0[0], t0[1], t1[0], t1[1]};
      qf[b][1] = u32x4{t2[0], t2[1], 0u, 0u};
      float n2 = 0.f;
#pragma unroll
      for (int i = 0; i < 4; ++i) { n2 = fmaf(bflo(qf[b][0][i]), bflo(qf[b][0][i]), n2); n2 = fmaf(bfhi(qf[b][0][i]), bfhi(qf[b][0][i]), n2); }
#pragma unroll
      for (int i = 0; i < 2; ++i) { n2 = fmaf(bflo(t2[i]), bflo(t2[i]), n2); n2 = fmaf(bfhi(t2[i]), bfhi(t2[i]), n2); }
      n2 = x32_add(n2);
      if (!(n2 == n2)) n2 = __builtin_inff();
      qn = fmaxf(qn, n2);
    }
    qn = wave_max(qn);
    const float kn = wave_max(__uint_as_float(knv[0]) + __uint_as_float(knv[1]));
    const unsigned fx = lds0 + 65536 + 10240;
    if (lane == 0) ldsw<u32x2>(fx + 8 * w, u32x2{__float_as_uint(qn), __float_as_uint(kn)});
    asm volatile("s_waitcnt vmcnt(0) lgkmcnt(0)\n\ts_barrier" ::: "memory");
    const u32x4 f0 = ldsr<u32x4>(fx), f1 = ldsr<u32x4>(fx + 16);
    const float qm = fmaxf(fmaxf(__uint_as_float(f0[0]), __uint_as_float(f0[2])), fmaxf(__uint_as_float(f1[0]), __uint_as_float(f1[2])));
    const float km = fmaxf(fmaxf(__uint_as_float(f0[1]), __uint_as_float(f0[3])), fmaxf(__uint_as_float(f1[1]), __uint_as_float(f1[3])));
    const int slow = rfl(!(qm * km <= 3600.f));
    f32x16 oa, ob;
#pragma unroll
    for (int i = 0; i < 16; ++i) { oa[i] = 0.f; ob[i] = 0.f; }
    STAMP(5); STAMPC(28);
    if (!slow) {
      attn_fast(kv, lds0, w, lane, qf[0][0], qf[0][1], qf[1][0], qf[1][1], oa, ob);
    }
    else {
      attn_slow(rs, lane, qf[0][0], qf[0][1], oa);
      attn_slow(rs, lane, qf[1][0], qf[1][1], ob);
    }
    STAMP(6); STAMPC(29);
    attn_store(rs, 2 * tg, 4 * hc + w, lane, oa);
    attn_store(rs, 2 * tg + 1, 4 * hc + w, lane, ob);
    publish(ws, WS_F3 + (tg * 8 + hc) * FS, tag);
    STAMP(7);
  } else {
    const int p = xcd * 32 + loc;
    const int pn = 4 * (xcd & 1) + (loc & 3), u = (xcd >> 1) + 4 * (loc >> 2);
    const int q4 = m16 >> 2, p4 = lane & 3;
    __builtin_amdgcn_s_setprio(3);
    u64 ep;
    asm volatile("global_load_dwordx2 %0, %1, %2 sc1" : "=v"(ep) : "v"(0u), "s"(ws) : "memory");
    u32x2 xr[5];
    {
      const __bf16* xp = x + (size_t)(8 * p + 2 * w + (lane >> 5)) * D_ + 4 * (lane & 31);
#pragma unroll
      for (int r = 0; r < 5; ++r) asm volatile("global_load_dwordx2 %0, %1, off offset:%2" : "=v"(xr[r]) : "v"(xp), "i"(256 * r) : "memory");
    }
    {
      const char* kb = (const char*)wk + 6400 * w; const char* vb = (const char*)wv + 6400 * w;
      const unsigned lk = lds0 + w * 12800;
#pragma unroll
      for (int i = 0; i < 6; ++i) { glds16(kb + i * 1024, lane * 16, lk + i * 1024); glds16(vb + i * 1024, lane * 16, lk + 6400 + i * 1024); }
      if (lane < 16) { glds16(kb + 6144, lane * 16, lk + 6144); glds16(vb + 6144, lane * 16, lk + 6400 + 6144); }
    }
    asm volatile("s_waitcnt vmcnt(14)" : "+v"(ep), "+v"(xr[0]), "+v"(xr[1]), "+v"(xr[2]), "+v"(xr[3]), "+v"(xr[4])::"memory");
    const u64 tag = mktag(ep);
    const unsigned xi = lds0 + 65536, pi = lds0 + 65536 + 10240;
    {
      float v[20], s = 0.f;
#pragma unroll
      for (int r = 0; r < 5; ++r) { v[4 * r] = bflo(xr[r][0]); v[4 * r + 1] = bfhi(xr[r][0]); v[4 * r + 2] = bflo(xr[r][1]); v[4 * r + 3] = bfhi(xr[r][1]); }
#pragma unroll
      for (int i = 0; i < 20; ++i) s += v[i];
      const float mean = x16_add(sum16(s)) * (1.f / D_);
      float s2 = 0.f;
#pragma unroll
      for (int i = 0; i < 20; ++i) { v[i] -= mean; s2 = fmaf(v[i], v[i], s2); }
      const float rstd = rsqrtf(x16_add(sum16(s2)) * (1.f / D_) + 1e-5f);
      const int i5 = lane & 31;
      const unsigned a = xi + (((i5 >> 3) * 4 + (i5 & 3)) * 8 + 2 * w + (lane >> 5)) * 16 + ((i5 >> 2) & 1) * 8;
#pragma unroll
      for (int r = 0; r < 5; ++r)
        ldsw<u32x2>(a + r * 2048, u32x2{cvtpk(v[4 * r] * rstd, v[4 * r + 1] * rstd), cvtpk(v[4 * r + 2] * rstd, v[4 * r + 3] * rstd)});
    }
    STAMP(1);
    WAITV(0);
    u32x4 ka[5][4];
    {
      const unsigned r0 = lds0 + w * 12800 + (4 * g4 + q4) * 40;
#pragma unroll
      for (int s = 0; s < 5; ++s) {
        ka[s][0] = trfrag(r0 + s * 1280 + p4 * 8, 640); ka[s][1] = trfrag(r0 + s * 1280 + 32, 640);
        ka[s][2] = trfrag(r0 + 6400 + s * 1280 + p4 * 8, 640); ka[s][3] = trfrag(r0 + 6400 + s * 1280 + 32, 640);
      }
    }
    BARRIER();
    {
      const unsigned gb = WS_XN + (p >> 1) * 20480 + (p & 1) * 128;
#pragma unroll
      for (int r = 0; r < 3; ++r) {
        const int c = tid + 256 * r;
        if (r < 2 || tid < 128) bst16(rs, ldsr<u32x4>(xi + c * 16), gb + (c >> 3) * 256 + (c & 7) * 16, 0);
      }
    }
    f32x4 kvp[4];
#pragma unroll
    for (int i = 0; i < 4; ++i) kvp[i] = f32x4{0.f, 0.f, 0.f, 0.f};
#pragma unroll
    for (int s = 0; s < 5; ++s) {
      const u32x4 b = ldsr<u32x4>(xi + (((5 * w + s) * 4 + g4) * 8 + (m16 & 7)) * 16);
      kvp[0] = mfma16(ka[s][0], b, kvp[0]); kvp[1] = mfma16(ka[s][1], b, kvp[1]);
      kvp[2] = mfma16(b, ka[s][2], kvp[2]); kvp[3] = mfma16(b, ka[s][3], kvp[3]);
    }
    if (m16 < 8) ldsw<f32x4>(pi + w * 1280 + (g4 * 8 + m16) * 16, kvp[0]);
    if (lane < 8) ldsw<f32x4>(pi + w * 1280 + (32 + lane) * 16, kvp[1]);
    if (lane < 32) ldsw<f32x4>(pi + w * 1280 + (40 + lane) * 16, kvp[2]);
    if (lane < 32 && m16 < 4) ldsw<f32x4>(pi + w * 1280 + (72 + g4 * 4 + m16) * 16, kvp[3]);
    publish(ws, WS_F0 + p * FS, tag);
    STAMP(2);
    const int c0 = (loc + xcd) % 5;
    u32x4 wvo;
    const char *pg = nullptr, *pu = nullptr;
    if (u < 27) {
      const int sc = ((((lane & 7) >> 1) ^ ((lane >> 4) & 3)) << 1) | (lane & 1);
#pragma unroll
      for (int i = 0; i < 4; ++i) wvo[i] = (8 * i + (lane >> 3)) * (F_ * 2) + sc * 16;
      pg = (const char*)(wg + (size_t)(32 * w) * F_ + 64 * u);
      pu = (const char*)(wu + (size_t)(32 * w) * F_ + 64 * u);
#pragma unroll
      for (int i = 0; i < 4; ++i) glds16(pg + c0 * 442368, wvo[i], lds0 + w * 8192 + i * 1024);
#pragma unroll
      for (int i = 0; i < 4; ++i) glds16(pu + c0 * 442368, wvo[i], lds0 + w * 8192 + 4096 + i * 1024);
    }
    {
      int cell = w == 0 ? g4 * 8 + (m16 & 7) : w == 1 ? 32 + (lane & 7) : w == 2 ? 40 + (lane & 31) : 72 + (g4 & 1) * 4 + (m16 & 3);
      f32x4 r = ldsr<f32x4>(pi + cell * 16);
#pragma unroll
      for (int sw = 1; sw < 4; ++sw) r += ldsr<f32x4>(pi + sw * 1280 + cell * 16);
      u32x2 v = {cvtpk(r[0], r[1]), cvtpk(r[2], r[3])};
      const unsigned kb = WS_KV + (p >> 2) * 4096;
      const int rho = 8 * (p & 3) + (m16 & 7);
      if (w < 2) {
        float n2 = bflo(v[0]) * bflo(v[0]) + bfhi(v[0]) * bfhi(v[0]) + bflo(v[1]) * bflo(v[1]) + bfhi(v[1]) * bfhi(v[1]);
        if (w == 0) n2 = x32_add(x16_add(n2));
        if (!(n2 == n2)) n2 = __builtin_inff();
        n2 = max16(n2);
        if (lane == 0) bst4(rs, __float_as_uint(n2), WS_KN + p * 8 + 4 * w, 0);
        if (w == 0) { if (m16 < 8) bst8(rs, v, kb + ((g4 & 1) * 32 + rho) * 16 + (g4 >> 1) * 8, 0); }
        else if (m16 < 8 && g4 < 2) bst16(rs, g4 ? u32x4{0u, 0u, 0u, 0u} : u32x4{v[0], v[1], 0u, 0u}, kb + 1024 + (g4 * 32 + rho) * 16, 0);
      } else {
        if (w == 3 && m16 >= 4) { const unsigned f = (m16 == 4 || m16 == 8) ? 0x3f803f80u : 0u; v = u32x2{f, f}; }
        if (lane < 32) bst8(rs, v, kb + 2048 + ((p >> 1) & 1) * 1024 + ((p & 1) * 32 + 16 * (w - 2) + m16) * 16 + g4 * 8, 0);
      }
    }
    publish(ws, WS_F1 + p * FS, tag);
    STAMP(3);
    if (u < 27) {
      poll(ws, WS_F0 + (32 * pn + 8 * w + (lane & 7)) * FS, tag);
      STAMP(4);
      u32x4 bvo, ra;
#pragma unroll
      for (int i = 0; i < 4; ++i) {
        bvo[i] = lane * 16 + i * 20480;
        ra[i] = lds0 + (4 * g4 + q4) * 128 + ((i ^ ((2 * g4 + (q4 >> 1)) & 3)) << 5) + p4 * 8;
      }
      f32x16 acc[8];
#pragma unroll
      for (int n = 0; n < 8; ++n)
#pragma unroll
        for (int i = 0; i < 16; ++i) acc[n][i] = 0.f;
      __builtin_amdgcn_s_setprio(GPRIO);
      gu_loop(pg, pu, ws + WS_XN + (16 * pn + 4 * w) * 20480, lds0 + w * 8192, bvo, wvo, ra, c0, acc);
      __builtin_amdgcn_s_setprio(3);
      STAMP(5);
#pragma unroll
      for (int tb = 0; tb < 4; ++tb)
#pragma unroll
        for (int k = 0; k < 2; ++k) {
          float hv[8];
#pragma unroll
          for (int i = 0; i < 8; ++i) hv[i] = silu_f(acc[2 * k + (i >> 2)][4 * tb + (i & 3)]) * acc[4 + 2 * k + (i >> 2)][4 * tb + (i & 3)];
          bst16(rs, u32x4{cvtpk(hv[0], hv[1]), cvtpk(hv[2], hv[3]), cvtpk(hv[4], hv[5]), cvtpk(hv[6], hv[7])}, lane * 16,
                WS_H + ((16 * pn + 4 * w + tb) * 54 + 2 * u + k) * 1024);
        }
    } else {
      const unsigned lw = lds0 + w * 8192;
      for (int id = (pn * 5 + (u - 27)) * 4 + w; id < 270; id += 160) {
        const int s = id / 5, fi = id % 5;
        const char* src = (const char*)(wd + (size_t)(32 * s) * D_ + 128 * fi);
#pragma unroll
        for (int i = 0; i < 8; ++i) glds16(src + i * (4 * D_ * 2), (lane >> 4) * (D_ * 2) + (lane & 15) * 16, lw + i * 1024);
        WAITV(0);
#pragma unroll
        for (int i = 0; i < 8; ++i) {
          const int nt = 8 * fi + i;
          bst16(rs, trfrag(lw + (4 * g4 + q4) * 256 + i * 32 + p4 * 8, 16 * 256), lane * 16, WS_WD + (((nt / 5) * 54 + s) * 5 + nt % 5) * 1024);
        }
        WAITL();
      }
    }
    publish(ws, WS_F2 + (pn * 32 + u) * FS, tag);
    STAMP(6);
    poll(ws, WS_F2 + ((lane < 40 ? lane / 5 : 0) * 32 + 27 + lane % 5) * FS, tag);
    poll(ws, WS_F2 + ((tg >> 2) * 32 + (lane & 31)) * FS, tag);
    STAMP(7);
    f32x4 hres[5];
    {
      const int s0 = 13 * w + (w < 2 ? w : 2);
      const unsigned sa = WS_WD + (hc * 54 + s0) * 5120, sb = WS_H + (4 * tg * 54 + s0) * 1024;
      u32x4 a[4][5], b[4][4];
      f32x4 acc[5][4];
#pragma unroll
      for (int n = 0; n < 5; ++n)
#pragma unroll
        for (int tb = 0; tb < 4; ++tb) acc[n][tb] = f32x4{0.f, 0.f, 0.f, 0.f};
      auto load = [&](int j) {
        const int jj = (j == 13 && w >= 2) ? 12 : j;
#pragma unroll
        for (int tb = 0; tb < 4; ++tb) b[j & 3][tb] = bld16(rs, lane * 16, sb + ((tb + w) & 3) * (54 * 1024) + jj * 1024);
#pragma unroll
        for (int n = 0; n < 5; ++n) a[j & 3][n] = bld16(rs, lane * 16 + (n & 3) * 1024, sa + jj * 5120 + (n >> 2) * 4096);
      };
      __builtin_amdgcn_s_setprio(GPRIO);
      {
      load(0); load(1); load(2);
#pragma unroll
      for (int j = 0; j < 14; ++j) {
        __builtin_amdgcn_sched_barrier(0);
        if (j + 3 < 14) load(j + 3);
        if (j < 13 || w < 2) {
#pragma unroll
          for (int n = 0; n < 5; ++n)
#pragma unroll
            for (int tb = 0; tb < 4; ++tb) acc[n][tb] = mfma16(a[j & 3][n], b[j & 3][tb], acc[n][tb]);
        }
      }
      }
      __builtin_amdgcn_s_setprio(3);
      STAMP(8);
      red4(acc, lds0, w, lane, hres);
    }
    unsigned hs[10];
#pragma unroll
    for (int n = 0; n < 5; ++n) { hs[2 * n] = cvtpk(silu_f(hres[n][0]), silu_f(hres[n][1])); hs[2 * n + 1] = cvtpk(silu_f(hres[n][2]), silu_f(hres[n][3])); }
    BARRIER();
    T80 t;
    t80_prefetch(t, wo + 80 * hc, lds0, w, lane);
    const size_t o = (size_t)(64 * tg + 16 * w + m16) * D_ + 80 * hc + 4 * g4;
    u32x2 xv[5];
#pragma unroll
    for (int n = 0; n < 5; ++n) xv[n] = *(const u32x2*)(x + o + 16 * n);
    STAMP(9);
    if (bid == 0) {
      poll(ws, WS_F2 + tid * FS, tag);
      poll(ws, WS_F3 + tid * FS, tag);
      BARRIER();
      if (tid == 0) __hip_atomic_store((GAS u64*)(ws + WS_E), tag, RLX_AGENT);
    } else {
      poll(ws, WS_F3 + (tg * 8 + (lane & 7)) * FS, tag);
    }
    WAITV(0);
    STAMP(10);
    f32x4 res[5];
    t80_compute(t, rs, WS_O + 4 * tg * 20480, lds0, w, lane, res);
#pragma unroll
    for (int n = 0; n < 5; ++n)
      *(u32x2*)(out + o + 16 * n) = u32x2{cvtpk(fmaf(res[n][0], bflo(hs[2 * n]), bflo(xv[n][0])), fmaf(res[n][1], bfhi(hs[2 * n]), bfhi(xv[n][0]))),
                                         cvtpk(fmaf(res[n][2], bflo(hs[2 * n + 1]), bflo(xv[n][1])), fmaf(res[n][3], bfhi(hs[2 * n + 1]), bfhi(xv[n][1])))};
    STAMP(11);
  }
  STAMPC(31);
}

extern "C" void kernel_launch(void* const* d_in, const int* in_sizes, int n_in, void* d_out, int out_size, void* d_ws, size_t ws_size,
                              hipStream_t stream) {
  (void)hipFuncSetAttribute((const void*)mega, hipFuncAttributeMaxDynamicSharedMemorySize, LDS_BYTES);
  mega<<<512, 256, LDS_BYTES, stream>>>((const __bf16*)d_in[0], (const __bf16*)d_in[1], (const __bf16*)d_in[2], (const __bf16*)d_in[3],
                                        (const __bf16*)d_in[4], (const __bf16*)d_in[6], (const __bf16*)d_in[7], (const __bf16*)d_in[8],
                                        (char*)d_ws, (__bf16*)d_out);
}
```

```cpp
#include <hip/hip_runtime.h>
#include <hip/hip_bf16.h>
#include <stdint.h>

typedef __bf16 bf16x8 __attribute__((ext_vector_type(8)));
typedef __bf16 bf16x16 __attribute__((ext_vector_type(16)));
typedef __bf16 bf16x2 __attribute__((ext_vector_type(2)));
typedef short s16x4 __attribute__((ext_vector_type(4)));
typedef float f32x16 __attribute__((ext_vector_type(16)));
typedef float f32x8 __attribute__((ext_vector_type(8)));
typedef float f32x4 __attribute__((ext_vector_type(4)));
typedef unsigned u32x4 __attribute__((ext_vector_type(4)));
typedef unsigned u32x2 __attribute__((ext_vector_type(2)));
typedef unsigned long long u64;
#define LAS __attribute__((address_space(3)))
#define GAS __attribute__((address_space(1)))
#if defined(__HIP_DEVICE_COMPILE__)
#define NOPK __attribute__((target("no-packed-fp32-ops")))
#else
#define NOPK
#endif
#define DI __device__ __forceinline__ NOPK
#define TIDX ((int)__builtin_amdgcn_workitem_id_x())
#define BIDX ((int)__builtin_amdgcn_workgroup_id_x())
#define u2f(x) __builtin_bit_cast(float, (unsigned)(x))
#define f2u(x) __builtin_bit_cast(unsigned, (float)(x))
#define STR_(x) #x
#define STR(x) STR_(x)

constexpr int S_ = 2048, D_ = 640, F_ = 1728;
constexpr float LOG2E = 1.4426950408889634f;
constexpr float QSCALE = 0.22360679774997896f * LOG2E;
constexpr int LDS_BYTES = 81920;
#define ASLEEP 0
#define PSLEEP 2
#define G2PRIO 0
#define G2DEPTH 3
#define ROT1 0
#define ROT2 0
#define ROTQ 0
#define ROTO 0
#define GLO0 9
#define GLO1 9
#define GPRIO 0
#define APRIO 1

constexpr unsigned WS_E = 0;
constexpr unsigned FS = 128;
constexpr unsigned WS_XN = 16384;
constexpr unsigned WS_KV = WS_XN + 128 * 20 * 1024;
constexpr unsigned WS_H = WS_KV + 64 * 4096;
constexpr unsigned WS_O = WS_H + 128 * 54 * 1024;
constexpr unsigned WS_WD = WS_O + 128 * 20 * 1024;
constexpr unsigned WS_F1 = WS_WD + 8 * 54 * 5 * 1024;
constexpr unsigned WS_F2 = WS_F1 + 256 * FS;
constexpr unsigned WS_F3 = WS_F2 + 256 * FS;
constexpr unsigned WS_END = WS_F3 + 256 * FS;

DI unsigned cvtpk(float lo, float hi) { bf16x2 v = {(__bf16)lo, (__bf16)hi}; return __builtin_bit_cast(unsigned, v); }
DI float bflo(unsigned u) { return u2f(u << 16); }
DI float bfhi(unsigned u) { return u2f(u & 0xffff0000u); }
DI f32x16 mfma32(u32x4 a, u32x4 b, f32x16 c) {
  return __builtin_amdgcn_mfma_f32_32x32x16_bf16(__builtin_bit_cast(bf16x8, a), __builtin_bit_cast(bf16x8, b), c, 0, 0, 0);
}
DI f32x4 mfma16(u32x4 a, u32x4 b, f32x4 c) {
  return __builtin_amdgcn_mfma_f32_16x16x32_bf16(__builtin_bit_cast(bf16x8, a), __builtin_bit_cast(bf16x8, b), c, 0, 0, 0);
}
DI u32x2 trread(unsigned addr) { return __builtin_bit_cast(u32x2, __builtin_amdgcn_ds_read_tr16_b64_v4i16((LAS s16x4*)(uintptr_t)addr)); }
DI u32x4 trfrag(unsigned addr, unsigned d) { u32x2 a = trread(addr), b = trread(addr + d); return u32x4{a[0], a[1], b[0], b[1]}; }
template <typename T> DI T ldsr(unsigned addr) { return *(LAS T*)(uintptr_t)addr; }
template <typename T> DI void ldsw(unsigned addr, T v) { *(LAS T*)(uintptr_t)addr = v; }
DI void glds16(const void* sbase, unsigned voff, unsigned lds) {
  asm volatile("s_mov_b32 m0, %2\n\ts_nop 0\n\tglobal_load_lds_dwordx4 %0, %1" ::"v"(voff), "s"(sbase), "s"(lds) : "memory");
}
DI void glds16_sc1(const void* sbase, unsigned voff, unsigned lds) {
  asm volatile("s_mov_b32 m0, %2\n\ts_nop 0\n\tglobal_load_lds_dwordx4 %0, %1 sc1" ::"v"(voff), "s"(sbase), "s"(lds) : "memory");
}
DI void touch(const void* sbase, unsigned voff, unsigned lds) {
  asm volatile("s_mov_b32 m0, %2\n\ts_nop 0\n\tglobal_load_lds_dword %0, %1" ::"v"(voff), "s"(sbase), "s"(lds) : "memory");
}
template <int CTRL> DI float dppf(float v) { return u2f(__builtin_amdgcn_update_dpp(0, f2u(v), CTRL, 0xf, 0xf, true)); }
DI float sum16(float v) {
  v += dppf<0xB1>(v); v += dppf<0x4E>(v); v += dppf<0x141>(v); v += dppf<0x140>(v); return v;
}
DI float max16(float v) {
  v = __builtin_fmaxf(v, dppf<0xB1>(v)); v = __builtin_fmaxf(v, dppf<0x4E>(v)); v = __builtin_fmaxf(v, dppf<0x141>(v)); v = __builtin_fmaxf(v, dppf<0x140>(v)); return v;
}
DI float x16_add(float v) { auto r = __builtin_amdgcn_permlane16_swap(f2u(v), f2u(v), false, false); return u2f(r[0]) + u2f(r[1]); }
DI float x16_max(float v) { auto r = __builtin_amdgcn_permlane16_swap(f2u(v), f2u(v), false, false); return __builtin_fmaxf(u2f(r[0]), u2f(r[1])); }
DI float x32_add(float v) { auto r = __builtin_amdgcn_permlane32_swap(f2u(v), f2u(v), false, false); return u2f(r[0]) + u2f(r[1]); }
DI float x32_max(float v) { auto r = __builtin_amdgcn_permlane32_swap(f2u(v), f2u(v), false, false); return __builtin_fmaxf(u2f(r[0]), u2f(r[1])); }
DI float wave_max(float v) { return x32_max(x16_max(max16(v))); }
DI int rfl(int v) { return __builtin_amdgcn_readfirstlane(v); }
#define WAITV(n) asm volatile("s_waitcnt vmcnt(" #n ")" ::: "memory")
#define WAITL() asm volatile("s_waitcnt lgkmcnt(0)" ::: "memory")
#define BARRIER() asm volatile("s_waitcnt lgkmcnt(0)\n\ts_barrier" ::: "memory")
#define RLX_AGENT __ATOMIC_RELAXED, __HIP_MEMORY_SCOPE_AGENT
typedef __amdgpu_buffer_rsrc_t rsrc_t;
DI u32x4 bld16(rsrc_t r, unsigned voff, unsigned soff) { return __builtin_amdgcn_raw_buffer_load_b128(r, voff, soff, 16); }
DI void bst16(rsrc_t r, u32x4 v, unsigned voff, unsigned soff) { __builtin_amdgcn_raw_buffer_store_b128(v, r, voff + soff, 0, 16); }
DI void bst8(rsrc_t r, u32x2 v, unsigned voff, unsigned soff) { __builtin_amdgcn_raw_buffer_store_b64(v, r, voff, soff, 16); }
DI void bst4(rsrc_t r, unsigned v, unsigned voff, unsigned soff) { __builtin_amdgcn_raw_buffer_store_b32(v, r, voff, soff, 16); }

#define STAMP(i)
#define STAMPC(i)
#define STAMP_DECL
#define STAMP_FLUSH()

DI u64 mktag(u64 ep) {
  const u64 e = ((u64)(unsigned)rfl((int)(ep >> 32)) << 32) | (unsigned)rfl((int)ep);
  u64 t = (e ^ 0x9E3779B97F4A7C15ull) * 0xD1342543DE82EF95ull + 1;
  t ^= t << 29;
  return t;
}
DI unsigned poll(const char* ws, unsigned off, u64 tag) {
  const GAS u64* p = (const GAS u64*)(ws + off);
  u64 v;
#pragma nounroll
  for (int spins = 0; spins < (1 << 16); ++spins) {
    v = __hip_atomic_load(p, RLX_AGENT);
    if (__builtin_amdgcn_ballot_w64(((v ^ tag) >> 16) != 0) == 0) break;
    __builtin_amdgcn_s_sleep(PSLEEP);
  }
  asm volatile("" ::: "memory");
  return (unsigned)v & 0xffffu;
}
#define DRAIN_BARRIER() asm volatile("s_waitcnt vmcnt(0) lgkmcnt(0)\n\ts_barrier" ::: "memory")
DI void publish(char* ws, unsigned off, u64 tag) {
  DRAIN_BARRIER();
  if (TIDX == 0) __hip_atomic_store((GAS u64*)(ws + off), tag, RLX_AGENT);
}

DI void t80_offsets(unsigned (&wv)[5], int lane) {
#pragma unroll
  for (int i = 0; i < 5; ++i) {
    const int byte = 1024 * i + 16 * lane, r = byte / 160;
    wv[i] = (8 * ((r >> 2) & 3) + 4 * (r >> 4) + (r & 3)) * (D_ * 2) + (byte - r * 160);
  }
}
DI void t80_stage(const char* wb, int g, const unsigned (&wv)[5], unsigned dst) {
#pragma unroll
  for (int i = 0; i < 5; ++i) glds16(wb + g * (64 * D_ * 2), wv[i], dst + i * 1024);
}
DI unsigned img_cell(int row, int c) { return row * 320 + ((c ^ ((row >> 1) & 3)) << 4); }
DI void red_spill(const f32x4 (&acc)[5][4], unsigned lds0, int w, int lane) {
  const unsigned d = lds0 + w * 20480 + lane * 16;
#pragma unroll
  for (int tb = 1; tb < 4; ++tb)
#pragma unroll
    for (int n = 0; n < 5; ++n) ldsw<f32x4>(d + (tb - 1) * 5120 + n * 1024, acc[n][tb]);
}
DI void red_gather(const f32x4 (&acc)[5][4], unsigned lds0, int w, int lane, f32x4 (&res)[5]) {
#pragma unroll
  for (int n = 0; n < 5; ++n) res[n] = acc[n][0];
#pragma unroll
  for (int tb = 1; tb < 4; ++tb) {
    const unsigned sa = lds0 + ((w - tb) & 3) * 20480 + (tb - 1) * 5120 + lane * 16;
#pragma unroll
    for (int n = 0; n < 5; ++n) res[n] += ldsr<f32x4>(sa + n * 1024);
  }
}

#define VEXP(n) "v_exp_f32 v[" STR(n) "], v[" STR(n) "]\n\t"
#define E4(b) VEXP(b) VEXP(b + 1) VEXP(b + 2) VEXP(b + 3)
#define QA0 "v[64:67]"
#define QA1 "v[68:71]"
#define QB0 "v[72:75]"
#define QB1 "v[76:79]"
#define OA "v[80:95]"
#define OB "v[96:111]"
#define VIDX "v208"
#define AK0 "v209"
#define AK1 "v210"
#define AV "v211"
#define GOFF "v212"
#define SR(b) "v[" STR(b) ":" STR(b + 15) "]"
#define SLO(b) "v[" STR(b) ":" STR(b + 7) "]"
#define SHI(b) "v[" STR(b + 8) ":" STR(b + 15) "]"
#define R4(b) "v[" STR(b) ":" STR(b + 3) "]"
#define R2(b) "v[" STR(b) ":" STR(b + 1) "]"
#define PER_A(Sc, Sn, Sp, X, Y, X0, X1)                                             \
  "v_mfma_f32_32x32x16_bf16 " SR(Sn) ", " R4(X) ", " QB0 ", 0\n\t"                  \
  X0                                                                                \
  E4(Sc)                                                                            \
  "v_mfma_f32_32x32x16_bf16 " SR(Sn) ", " R4(X + 4) ", " QB1 ", " SR(Sn) "\n\t"     \
  X1                                                                                \
  E4(Sc + 4)                                                                        \
  "v_smfmac_f32_32x32x32_bf16 " OB ", " R4(Y + 12) ", " SHI(Sp) ", " VIDX "\n\t"    \
  "s_waitcnt lgkmcnt(0)\n\t"                                                        \
  E4(Sc + 8)                                                                        \
  "v_smfmac_f32_32x32x32_bf16 " OA ", " R4(X + 8) ", " SLO(Sc) ", " VIDX "\n\t"     \
  E4(Sc + 12)
#define PER_B(Sc, Sn, Sp, X, Y, KO, VO)                                             \
  "v_mfma_f32_32x32x16_bf16 " SR(Sn) ", " R4(Y) ", " QA0 ", 0\n\t"                  \
  "s_mov_b64 exec, %[vm]\n\t"                                                       \
  "ds_read_b128 " R4(Y + 8) ", " AV " offset:" STR(VO) "\n\t"                       \
  "ds_read_b128 " R4(Y + 12) ", " AV " offset:" STR(VO + 640) "\n\t"                \
  "s_mov_b64 exec, -1\n\t"                                                          \
  E4(Sc)                                                                            \
  "v_mfma_f32_32x32x16_bf16 " SR(Sn) ", " R4(Y + 4) ", " QA1 ", " SR(Sn) "\n\t"     \
  "ds_read_b128 " R4(X) ", " AK0 " offset:" STR(KO) "\n\t"                          \
  E4(Sc + 4)                                                                        \
  "v_smfmac_f32_32x32x32_bf16 " OA ", " R4(X + 12) ", " SHI(Sp) ", " VIDX "\n\t"    \
  "ds_read_b64 " R2(X + 4) ", " AK1 " offset:" STR(KO) "\n\t"                       \
  E4(Sc + 8)                                                                        \
  "v_smfmac_f32_32x32x32_bf16 " OB ", " R4(X + 8) ", " SLO(Sc) ", " VIDX "\n\t"     \
  E4(Sc + 12)
#define GL(b, o) "global_load_lds_dwordx4 " GOFF ", " b " offset:" STR(o) " sc1\n\t"
#define TILE_E(t, KO, VO, X0, X1) PER_A(112, 128, 160, 176, 192, X0, X1) PER_B(128, 144, 112, 176, 192, KO, VO)
#define TILE_O(t, KO, VO, X0, X1) PER_A(144, 160, 128, 192, 176, X0, X1) PER_B(160, 112, 144, 192, 176, KO, VO)
#define GROUP(L0, L1, L2, L3, L4, WV)                                                                                \
  TILE_E(0, 5120, 2560, "s_add_u32 %[mt], %[cnt], 2\n\ts_and_b32 %[mt], %[mt], 3\n\t", "s_mul_i32 %[mt], %[mt], 20480\n\ts_add_u32 m0, %[mt], %[mb]\n\t") \
  TILE_O(1, 7680, 5120, L0, "")                                                                                      \
  TILE_E(2, 10240, 7680, L1, "")                                                                                     \
  TILE_O(3, 12800, 10240, L2, "")                                                                                    \
  TILE_E(4, 15360, 12800, L3, "s_add_u32 m0, m0, 0x1000\n\ts_and_b32 %[mt], %[cnt], 3\n\ts_cmp_eq_u32 %[mt], 3\n\t") \
  TILE_O(5, 17920, 15360, L4, "s_cselect_b32 %[mt], 0xffff1000, %[c20]\n\ts_add_u32 %[cnt], %[cnt], 1\n\t")          \
  PER_A(112, 128, 160, 176, 192, "v_add_u32 " AK0 ", %[mt], " AK0 "\n\t", "v_add_u32 " AK1 ", %[mt], " AK1 "\n\t")   \
  "s_waitcnt vmcnt(" STR(WV) ")\n\t"                                                                                 \
  "s_barrier\n\t"                                                                                                    \
  PER_B(128, 144, 112, 176, 192, 0, 17920)                                                                           \
  TILE_O(7, 2560, 0, "v_add_u32 " AV ", %[mt], " AV "\n\t", "v_add_u32 " GOFF ", %[c20], " GOFF "\n\t")

DI void attn_fast(const char* kv, unsigned lds0, int w, int lane, u32x4 qa0, u32x4 qa1, u32x4 qb0, u32x4 qb1, f32x16& oa, f32x16& ob) {
  const char* gnext = kv + 2 * 20480 + w * 5120;
  const int r = lane & 31;
  unsigned ak0 = lds0 + lane * 16, ak1 = lds0 + 1024 + r * 8, av = lds0 + 1280 + (lane >> 5) * 320 + r * 16, goff = lane * 16;
  const unsigned vc = (r == 20 || r == 24) ? 0x3f803f80u : 0u;
  const unsigned mbase = lds0 + w * 5120;
  const u64 vm = 0x000fffff000fffffull;
  unsigned cnt, mtmp;
  asm volatile(
      "v_mov_b32 v184, %[vc]\n\tv_mov_b32 v185, %[vc]\n\tv_mov_b32 v186, %[vc]\n\tv_mov_b32 v187, %[vc]\n\t"
      "v_mov_b32 v188, %[vc]\n\tv_mov_b32 v189, %[vc]\n\tv_mov_b32 v190, %[vc]\n\tv_mov_b32 v191, %[vc]\n\t"
      "ds_read_b128 v[176:179], " AK0 "\n\t"
      "ds_read_b64 v[180:181], " AK1 "\n\t"
      "ds_read_b128 v[192:195], " AK0 " offset:2560\n\t"
      "ds_read_b64 v[196:197], " AK1 " offset:2560\n\t"
      "s_mov_b64 exec, %[vm]\n\t"
      "ds_read_b128 v[184:187], " AV "\n\t"
      "ds_read_b128 v[188:191], " AV " offset:640\n\t"
      "s_mov_b64 exec, -1\n\t"
      "v_mov_b32 v200, %[vc]\n\tv_mov_b32 v201, %[vc]\n\tv_mov_b32 v202, %[vc]\n\tv_mov_b32 v203, %[vc]\n\t"
      "v_mov_b32 v204, %[vc]\n\tv_mov_b32 v205, %[vc]\n\tv_mov_b32 v206, %[vc]\n\tv_mov_b32 v207, %[vc]\n\t"
      "v_mov_b32 v182, 0\n\tv_mov_b32 v183, 0\n\tv_mov_b32 v198, 0\n\tv_mov_b32 v199, 0\n\t"
      "v_mov_b32 " VIDX ", 0xdddd\n\t"
      "v_mov_b32 v168, 0\n\tv_mov_b32 v169, 0\n\tv_mov_b32 v170, 0\n\tv_mov_b32 v171, 0\n\t"
      "v_mov_b32 v172, 0\n\tv_mov_b32 v173, 0\n\tv_mov_b32 v174, 0\n\tv_mov_b32 v175, 0\n\t"
      "s_mov_b32 %[cnt], 0\n\t"
      "s_waitcnt lgkmcnt(0)\n\t"
      "v_mfma_f32_32x32x16_bf16 " SR(112) ", v[176:179], " QA0 ", 0\n\t"
      "v_mfma_f32_32x32x16_bf16 " SR(112) ", v[180:183], " QA1 ", " SR(112) "\n\t"
      "s_nop 7\n\ts_nop 7\n\t"
      "1:\n\t"
      GROUP(GL("%[gn]", 0), GL("%[gn]", 1024), GL("%[gn]", 2048), GL("%[gn]", 3072), GL("%[gn2]", 0), 5)
      "s_cmp_lt_u32 %[cnt], 6\n\t"
      "s_cbranch_scc1 1b\n\t"
      "2:\n\t"
      GROUP("", "", "", "", "", 0)
      "s_cmp_lt_u32 %[cnt], 8\n\t"
      "s_cbranch_scc1 2b\n\t"
      "v_smfmac_f32_32x32x32_bf16 " OB ", v[204:207], " SHI(160) ", " VIDX "\n\t"
      "s_waitcnt lgkmcnt(0)\n\t"
      "s_nop 7\n\ts_nop 7\n\t"
      : "+{v[80:95]}"(oa), "+{v[96:111]}"(ob), "+{v209}"(ak0), "+{v210}"(ak1), "+{v211}"(av), "+{v212}"(goff), [cnt] "=&s"(cnt), [mt] "=&s"(mtmp)
      : "{v[64:67]}"(qa0), "{v[68:71]}"(qa1), "{v[72:75]}"(qb0), "{v[76:79]}"(qb1), [gn] "s"(gnext), [gn2] "s"(gnext + 4096), [mb] "s"(mbase), [vm] "s"(vm), [vc] "v"(vc),
        [c20] "s"(20480)
      : "memory", "scc",
        "v112", "v113", "v114", "v115", "v116", "v117", "v118", "v119", "v120", "v121", "v122", "v123", "v124", "v125", "v126", "v127",
        "v128", "v129", "v130", "v131", "v132", "v133", "v134", "v135", "v136", "v137", "v138", "v139", "v140", "v141", "v142", "v143",
        "v144", "v145", "v146", "v147", "v148", "v149", "v150", "v151", "v152", "v153", "v154", "v155", "v156", "v157", "v158", "v159",
        "v160", "v161", "v162", "v163", "v164", "v165", "v166", "v167", "v168", "v169", "v170", "v171", "v172", "v173", "v174", "v175",
        "v176", "v177", "v178", "v179", "v180", "v181", "v182", "v183", "v184", "v185", "v186", "v187", "v188", "v189", "v190", "v191",
        "v192", "v193", "v194", "v195", "v196", "v197", "v198", "v199", "v200", "v201", "v202", "v203", "v204", "v205", "v206", "v207",
        "v208");
}

DI f32x16 slow_scores(rsrc_t rs, int lane, unsigned so, u32x4 q0, u32x4 q1) {
  f32x16 z;
#pragma unroll
  for (int i = 0; i < 16; ++i) z[i] = 0.f;
  u32x2 k1 = __builtin_amdgcn_raw_buffer_load_b64(rs, 1024 + (lane & 31) * 8, so, 16);
  if (lane >= 32) k1 = u32x2{0u, 0u};
  return mfma32(u32x4{k1[0], k1[1], 0u, 0u}, q1, mfma32(bld16(rs, lane * 16, so), q0, z));
}
DI void attn_slow(rsrc_t rs, int lane, u32x4 q0, u32x4 q1, f32x16& o) {
  float m = -__builtin_inff();
#pragma nounroll
  for (int kt = 0; kt < 64; ++kt) {
    const f32x16 s = slow_scores(rs, lane, WS_KV + kt * 2560, q0, q1);
#pragma unroll
    for (int i = 0; i < 16; ++i) m = __builtin_fmaxf(m, s[i]);
  }
  m = x32_max(m);
  const int r = lane & 31;
  const unsigned vc = (r == 20 || r == 24) ? 0x3f803f80u : 0u, va = 1280 + (lane >> 5) * 320 + (r < 20 ? r : 0) * 16;
#pragma nounroll
  for (int kt = 0; kt < 64; ++kt) {
    const unsigned so = WS_KV + kt * 2560;
    const f32x16 s = slow_scores(rs, lane, so, q0, q1);
    u32x4 v0 = bld16(rs, va, so), v1 = bld16(rs, va + 640, so);
    if (r >= 20) { v0 = u32x4{vc, vc, vc, vc}; v1 = v0; }
    f32x8 lo, hi;
#pragma unroll
    for (int i = 0; i < 8; ++i) { lo[i] = __builtin_amdgcn_exp2f(s[i] - m); hi[i] = __builtin_amdgcn_exp2f(s[8 + i] - m); }
    o = __builtin_amdgcn_smfmac_f32_32x32x32_bf16(__builtin_bit_cast(bf16x8, v0), __builtin_bit_cast(bf16x16, lo), o, 0xdddd, 0, 0);
    o = __builtin_amdgcn_smfmac_f32_32x32x32_bf16(__builtin_bit_cast(bf16x8, v1), __builtin_bit_cast(bf16x16, hi), o, 0xdddd, 0, 0);
  }
}

DI void attn_stage(unsigned oi, int b, int w, int lane, const f32x16& o) {
  const int h = lane >> 5, m = lane & 31;
  const float rl = __builtin_amdgcn_rcpf(h ? o[8] : o[12]);
  const unsigned tb = oi + (2 * b + (m >> 4)) * 2560 + (m & 15) * 16;
#pragma unroll
  for (int g = 0; g < 3; ++g) {
    const int c = 5 * w + 2 * g + h;
    const u32x2 v = {cvtpk(o[4 * g] * rl, o[4 * g + 1] * rl), cvtpk(o[4 * g + 2] * rl, o[4 * g + 3] * rl)};
    if (g < 2 || !h) ldsw<u32x2>(tb + (c >> 1) * 256 + (c & 1) * 8, v);
  }
}

#define VR4(b) "v[" STR(b) ":" STR(b + 3) "]"
#define VR2(b) "v[" STR(b) ":" STR(b + 1) "]"
#define G_M1(n, ab, bs, t) "v_mfma_f32_16x16x32_bf16 " VR4(128 + (n) * 16 + (t) * 4) ", " VR4(48 + (ab) * 4) ", " VR4(64 + (bs) * 16 + (t) * 4) ", " VR4(128 + (n) * 16 + (t) * 4) "\n\t"
#define G_R1(f, t) "ds_read_b64_tr_b16 " VR2(48 + ((f) & 3) * 4 + (t) * 2) ", v[44+((" STR(f) ")&3)] offset:" STR(((f) >> 2) * 4096 + (t) * 2048) "\n\t"
#define G_RD(f) G_R1(f, 0) G_R1(f, 1)
#define G_SLOT(f, R0, R1, X, W) G_M1((f) & 7, (f) & 3, (f) >> 3, 0) R0 G_M1((f) & 7, (f) & 3, (f) >> 3, 1) R1 G_M1((f) & 7, (f) & 3, (f) >> 3, 2) X G_M1((f) & 7, (f) & 3, (f) >> 3, 3) W
#define G_S(f, X) G_SLOT(f, G_R1(f + 3, 0), G_R1(f + 3, 1), X, "s_waitcnt lgkmcnt(4)\n\t")
#define G_SV(f, X, nv) G_SLOT(f, G_R1(f + 3, 0), G_R1(f + 3, 1), X, "s_waitcnt vmcnt(" STR(nv) ") lgkmcnt(4)\n\t")
#define G_BL(set, tb, base, off) "global_load_dwordx4 " VR4(64 + (set) * 16 + (tb) * 4) ", v[32+" STR(tb) "], " base " offset:" STR(off) " sc1\n\t"
#define G_WL(rb, base) "global_load_lds_dwordx4 v[36+" STR(rb) "], " base "\n\ts_add_u32 m0, m0, 0x400\n\t"
#define G_STEP(j, X0, X1, X2, X3, X4, X5, X6, X7, nv) G_S(8 * j, X0) G_S(8 * j + 1, X1) G_S(8 * j + 2, X2) G_S(8 * j + 3, X3) G_S(8 * j + 4, X4) G_S(8 * j + 5, X5) G_S(8 * j + 6, X6) G_SV(8 * j + 7, X7, nv)
#define G_LAST(X0, X1, X2, X3, X5, X6, X7) G_S(24, X0) G_S(25, X1) G_S(26, X2) G_S(27, X3) G_S(28, "") G_SLOT(29, "", "", X5, "s_waitcnt lgkmcnt(2)\n\t") G_SLOT(30, "", "", X6, "s_waitcnt lgkmcnt(0)\n\t") G_SLOT(31, "", "", X7, "")
#define G_HEAD G_RD(0) G_RD(1) G_RD(2) "s_waitcnt lgkmcnt(4)\n\t"

DI void gu_loop(const char* pg, const char* pu, const char* px, unsigned m0base, u32x4 bvo, u32x4 wvo, u32x4 ra, int c0, f32x16 (&acc)[8]) {
  unsigned cnt, grp, t0;
  u64 sg, su, sx, sn;
  asm volatile(
      "s_waitcnt lgkmcnt(0)\n\t"
      "s_lshl_b32 %[t0], %[c0], 12\n\t"
      "s_mov_b64 %[sx], %[px]\n\t"
      "s_mov_b32 %[grp], %[c0]\n\t"
      "s_add_u32 s40, s40, %[t0]\n\ts_addc_u32 s41, s41, 0\n\t"
      G_BL(0, 0, "%[sx]", 0) G_BL(0, 1, "%[sx]", 0) G_BL(0, 2, "%[sx]", 0) G_BL(0, 3, "%[sx]", 0)
      G_BL(1, 0, "%[sx]", 1024) G_BL(1, 1, "%[sx]", 1024) G_BL(1, 2, "%[sx]", 1024) G_BL(1, 3, "%[sx]", 1024)
      G_BL(2, 0, "%[sx]", 2048) G_BL(2, 1, "%[sx]", 2048) G_BL(2, 2, "%[sx]", 2048) G_BL(2, 3, "%[sx]", 2048)
      "s_mov_b32 %[cnt], 0\n\t"
      "1:\n\t"
      "s_add_u32 %[grp], %[grp], 1\n\t"
      "s_cmp_eq_u32 %[grp], 5\n\t"
      "s_cselect_b32 %[grp], 0, %[grp]\n\t"
      "s_mov_b64 %[sg], %[pg]\n\ts_mov_b64 %[su], %[pu]\n\ts_mov_b64 %[sn], %[px]\n\t"
      "s_mul_i32 %[t0], %[grp], 442368\n\t"
      "s_add_u32 s36, s36, %[t0]\n\ts_addc_u32 s37, s37, 0\n\t"
      "s_add_u32 s38, s38, %[t0]\n\ts_addc_u32 s39, s39, 0\n\t"
      "s_lshl_b32 %[t0], %[grp], 12\n\t"
      "s_add_u32 s42, s42, %[t0]\n\ts_addc_u32 s43, s43, 0\n\t"
      "s_and_b32 %[t0], %[cnt], 1\n\t"
      "s_xor_b32 %[t0], %[t0], 1\n\t"
      "s_lshl_b32 %[t0], %[t0], 15\n\t"
      "s_add_u32 m0, %[t0], %[mb]\n\t"
      "s_cmp_ge_u32 %[cnt], " STR(GLO0) "\n\t"
      "s_cbranch_scc0 3f\n\t"
      "s_setprio 2\n\t"
      "3:\n\t"
      "s_cmp_ge_u32 %[cnt], " STR(GLO1) "\n\t"
      "s_cbranch_scc0 4f\n\t"
      "s_setprio 0\n\t"
      "4:\n\t"
      "s_waitcnt vmcnt(8)\n\t"
      "s_barrier\n\t"
      G_HEAD
      G_STEP(0, G_WL(0, "%[sg]"), G_WL(1, "%[sg]"), G_WL(2, "%[sg]"), G_WL(3, "%[sg]"), G_BL(3, 0, "%[sx]", 3072), G_BL(3, 1, "%[sx]", 3072), G_BL(3, 2, "%[sx]", 3072), G_BL(3, 3, "%[sx]", 3072), 12)
      G_STEP(1, G_WL(0, "%[su]"), G_WL(1, "%[su]"), G_WL(2, "%[su]"), G_WL(3, "%[su]"), G_BL(0, 0, "%[sn]", 0), G_BL(0, 1, "%[sn]", 0), G_BL(0, 2, "%[sn]", 0), G_BL(0, 3, "%[sn]", 0), 16)
      G_STEP(2, G_BL(1, 0, "%[sn]", 1024), "", G_BL(1, 1, "%[sn]", 1024), "", G_BL(1, 2, "%[sn]", 1024), "", G_BL(1, 3, "%[sn]", 1024), "", 12)
      G_LAST(G_BL(2, 0, "%[sn]", 2048), G_BL(2, 1, "%[sn]", 2048), G_BL(2, 2, "%[sn]", 2048), G_BL(2, 3, "%[sn]", 2048),
             "v_xor_b32 v44, 0x8000, v44\n\tv_xor_b32 v45, 0x8000, v45\n\t", "v_xor_b32 v46, 0x8000, v46\n\tv_xor_b32 v47, 0x8000, v47\n\t", "s_mov_b64 %[sx], %[sn]\n\t")
      "s_add_u32 %[cnt], %[cnt], 1\n\t"
      "s_cmp_lt_u32 %[cnt], 4\n\t"
      "s_cbranch_scc1 1b\n\t"
      "s_waitcnt vmcnt(8)\n\t"
      "s_barrier\n\t"
      G_HEAD
      G_STEP(0, G_BL(3, 0, "%[sx]", 3072), G_BL(3, 1, "%[sx]", 3072), G_BL(3, 2, "%[sx]", 3072), G_BL(3, 3, "%[sx]", 3072), "", "", "", "", 8)
      G_STEP(1, "", "", "", "", "", "", "", "", 4)
      G_STEP(2, "", "", "", "", "", "", "", "", 0)
      G_LAST("", "", "", "", "", "", "")
      "s_nop 7\n\ts_nop 3\n\t"
      : "+{v[128:143]}"(acc[0]), "+{v[144:159]}"(acc[1]), "+{v[160:175]}"(acc[2]), "+{v[176:191]}"(acc[3]),
        "+{v[192:207]}"(acc[4]), "+{v[208:223]}"(acc[5]), "+{v[224:239]}"(acc[6]), "+{v[240:255]}"(acc[7]),
        "+{v[44:47]}"(ra), [cnt] "=&s"(cnt), [grp] "=&s"(grp), [t0] "=&s"(t0),
        [sg] "=&{s[36:37]}"(sg), [su] "=&{s[38:39]}"(su), [sx] "=&{s[40:41]}"(sx), [sn] "=&{s[42:43]}"(sn)
      : "{v[32:35]}"(bvo), "{v[36:39]}"(wvo), [pg] "s"(pg), [pu] "s"(pu), [px] "s"(px), [mb] "s"(m0base), [c0] "s"(c0)
      : "memory", "scc",
        "v48", "v49", "v50", "v51", "v52", "v53", "v54", "v55", "v56", "v57", "v58", "v59", "v60", "v61", "v62", "v63",
        "v64", "v65", "v66", "v67", "v68", "v69", "v70", "v71", "v72", "v73", "v74", "v75", "v76", "v77", "v78", "v79",
        "v80", "v81", "v82", "v83", "v84", "v85", "v86", "v87", "v88", "v89", "v90", "v91", "v92", "v93", "v94", "v95",
        "v96", "v97", "v98", "v99", "v100", "v101", "v102", "v103", "v104", "v105", "v106", "v107", "v108", "v109", "v110", "v111",
        "v112", "v113", "v114", "v115", "v116", "v117", "v118", "v119", "v120", "v121", "v122", "v123", "v124", "v125", "v126", "v127");
}

DI float silu_f(float g) { return g * __builtin_amdgcn_rcpf(1.f + __builtin_amdgcn_exp2f(-LOG2E * g)); }

NOPK __global__ void __launch_bounds__(256, 2)
mega(const __bf16* __restrict__ x, const __bf16* __restrict__ wq, const __bf16* __restrict__ wk, const __bf16* __restrict__ wv,
     const __bf16* __restrict__ wo, const __bf16* __restrict__ wg, const __bf16* __restrict__ wu, const __bf16* __restrict__ wd,
     char* __restrict__ ws, __bf16* __restrict__ out) {
  extern __shared__ __attribute__((aligned(1024))) char smem[];
  const int tid = TIDX, lane = tid & 63, w = rfl(tid >> 6);
  const unsigned lds0 = (unsigned)(uintptr_t)(LAS char*)smem;
  const int bid = BIDX & 255, xcd = bid & 7, loc = bid >> 3;
  const int tg = 4 * xcd + (loc & 3), hc = loc >> 2;
  const rsrc_t rs = __builtin_amdgcn_make_buffer_rsrc(ws, 0, WS_END, 0x00020000);
  STAMP_DECL;
  STAMP(0); STAMPC(30);
  const int g4 = lane >> 4, m16 = lane & 15;

  if (BIDX < 256) {
    int tid2 = TIDX; asm volatile("" : "+v"(tid2));
    const int lane = tid2 & 63, g4 = lane >> 4, m16 = lane & 15;
    __builtin_amdgcn_s_setprio(APRIO);
    u64 ep;
    asm volatile("global_load_dwordx2 %0, %1, %2 sc1" : "=v"(ep) : "v"(0u), "s"(ws) : "memory");
    const int th = w & 1, kh = w >> 1;
    {
      unsigned wv5[5];
      t80_offsets(wv5, lane);
      const char* wb = (const char*)(wq + (size_t)(320 * kh + 32 * th) * D_ + 80 * hc);
      const char* xb = (const char*)(x + (size_t)(64 * tg + 32 * th) * D_ + 320 * kh);
      int gk[5];
#pragma unroll
      for (int i = 0; i < 5; ++i) gk[i] = ROTQ ? (i + (loc & 3) + hc) % 5 : i;
      unsigned xo[4], ra[2];
#pragma unroll
      for (int j = 0; j < 4; ++j) xo[j] = (16 * (j & 1) + (lane >> 2)) * (D_ * 2) + (lane & 3) * 16 + (((j >> 1) ^ th) << 6);
#pragma unroll
      for (int j = 0; j < 2; ++j) ra[j] = lds0 + (2 * kh + (j ^ th)) * 5120 + (4 * g4 + (m16 >> 2)) * 160 + (lane & 3) * 8;
      const int bp = (4 * m16 + g4) * 4;
      u32x4 b[5][4], aa[2][5], bb[2][2];
#define QLD(i_) do { _Pragma("unroll") for (int j = 0; j < 4; ++j) \
        asm volatile("global_load_dwordx4 %0, %1, %2" : "=v"(b[i_][j]) : "v"(xo[j]), "s"(xb + gk[i_] * 128) : "memory"); \
        t80_stage(wb, gk[i_], wv5, lds0 + ((i_) & 3) * 20480 + w * 5120); } while (0)
#define QWAIT(n_, i_) do { __builtin_amdgcn_sched_barrier(0); \
        asm volatile("s_waitcnt vmcnt(" #n_ ") lgkmcnt(0)\n\ts_barrier" : "+v"(b[i_][0]), "+v"(b[i_][1]), "+v"(b[i_][2]), "+v"(b[i_][3])::"memory"); } while (0)
#define QRD(i_, j_) do { _Pragma("unroll") for (int t = 0; t < 2; ++t) _Pragma("unroll") for (int e = 0; e < 4; ++e) \
          bb[j_][t][e] = __builtin_amdgcn_ds_bpermute(bp, b[i_][2 * (j_) + t][e]); \
        _Pragma("unroll") for (int n = 0; n < 5; ++n) aa[j_][n] = trfrag(ra[j_] + ((i_) & 3) * 20480 + n * 32, 16 * 160); } while (0)
#define QM1(k_) do { constexpr int j = (k_) >= 19, q = (k_) - 19 * j;                                                   \
        if (q < 4) { if (q & 1) asq[q >> 1] = mfma16(bb[j][q >> 1], bb[j][q >> 1], asq[q >> 1]); else asum[q >> 1] = mfma16(ones, bb[j][q >> 1], asum[q >> 1]); } \
        else if (q < 14) acc[(q - 4) >> 1][q & 1] = mfma16(aa[j][(q - 4) >> 1], bb[j][q & 1], acc[(q - 4) >> 1][q & 1]); \
        else acs[(q - 14) % 5] = mfma16(aa[j][(q - 14) % 5], ones, acs[(q - 14) % 5]); } while (0)
#define QL1(l_, k_) do { if ((l_) < 5) { if ((k_) < 4) asm volatile("global_load_dwordx4 %0, %1, %2" : "=v"(b[(l_) % 5][(k_) & 3]) : "v"(xo[(k_) & 3]), "s"(xb + gk[(l_) % 5] * 128)); \
        else asm volatile("s_mov_b32 m0, %2\n\ts_nop 0\n\tglobal_load_lds_dwordx4 %0, %1" ::"v"(wv5[((k_) + 1) % 5]), "s"(wb + gk[(l_) % 5] * (64 * D_ * 2)), \
                          "s"(lds0 + ((l_) & 3) * 20480 + w * 5120 + (((k_) + 1) % 5) * 1024)); } } while (0)
#define QBP(i_, t_) _Pragma("unroll") for (int e = 0; e < 4; ++e) bb[1][t_][e] = __builtin_amdgcn_ds_bpermute(bp, b[i_][2 + (t_)][e])
#define QTR(i_, n_) aa[1][n_] = trfrag(ra[1] + ((i_) & 3) * 20480 + (n_) * 32, 16 * 160)
#define SB0 __builtin_amdgcn_sched_barrier(0)
#define QGRP(i_) do { QRD(i_, 0); SB0;                                                                                \
        QL1(i_ + 2, 0); QBP(i_, 0); QM1(0); QM1(1); QM1(2); QM1(3); SB0;                                              \
        QL1(i_ + 2, 1); QBP(i_, 1); QM1(4); QM1(5); QM1(6); QM1(7); SB0;                                              \
        QL1(i_ + 2, 2); QTR(i_, 0); QTR(i_, 1); QM1(8); QM1(9); QM1(10); QM1(11); SB0;                                \
        QL1(i_ + 2, 3); QTR(i_, 2); QTR(i_, 3); QTR(i_, 4); QM1(12); QM1(13); QM1(14); QM1(15); SB0;                  \
        QL1(i_ + 2, 4); QM1(16); QM1(17); QM1(18); QM1(19); SB0;                                                      \
        QL1(i_ + 2, 5); QM1(20); QM1(21); QM1(22); QM1(23); SB0;                                                      \
        QL1(i_ + 2, 6); QM1(24); QM1(25); QM1(26); SB0;                                                               \
        QL1(i_ + 2, 7); QM1(27); QM1(28); QM1(29); SB0;                                                               \
        QL1(i_ + 2, 8); QM1(30); QM1(31); QM1(32); SB0; } while (0)
      __builtin_amdgcn_s_sleep(ASLEEP);
      QLD(0); QLD(1);
      STAMP(8);
      const u32x4 ones = {0x3f803f80u, 0x3f803f80u, 0x3f803f80u, 0x3f803f80u};
      const f32x4 z4 = {0.f, 0.f, 0.f, 0.f};
      f32x4 acc[5][2], acs[5], asum[2] = {z4, z4}, asq[2] = {z4, z4};
#pragma unroll
      for (int n = 0; n < 5; ++n) { acc[n][0] = z4; acc[n][1] = z4; acs[n] = z4; }
      QWAIT(9, 0); asm volatile("" : "+v"(ep));
      STAMP(1);
      QGRP(0);
      QWAIT(9, 1); QGRP(1);
      STAMP(9);
      QWAIT(9, 2); QGRP(2);
      QWAIT(9, 3); QGRP(3);
      QWAIT(0, 4); QGRP(4);
      STAMP(2);
#pragma unroll
      for (int t = 0; t < 2; ++t) {
        const int row = 32 * th + 16 * t + m16;
#pragma unroll
        for (int n = 0; n < 5; ++n) ldsw<f32x4>(lds0 + (2 + kh) * 20480 + img_cell(row, 4 * n + g4), acc[n][t]);
        const float d = (m16 & 2) ? ((m16 & 1) ? asq[t][3] : asq[t][2]) : ((m16 & 1) ? asq[t][1] : asq[t][0]);
        if (g4 == (m16 >> 2)) ldsw<u32x2>(lds0 + 20480 + row * 16 + kh * 8, u32x2{f2u(asum[t][0]), f2u(d)});
      }
      if (m16 == 0) {
#pragma unroll
        for (int n = 0; n < 5; ++n) ldsw<f32x4>(lds0 + 21504 + w * 320 + n * 64 + g4 * 16, acs[n]);
      }
    }
    const u64 tag = mktag(ep);
    STAMP(3);
    const unsigned knb = poll(ws, WS_F1 + (32 * (lane >> 3) + 8 * w + (lane & 7)) * FS, tag);
    STAMP(4);
    BARRIER();
    const char* kv = ws + WS_KV;
#pragma unroll
    for (int i = 0; i < 5; ++i) glds16_sc1(kv + w * 5120 + i * 1024, lane * 16, lds0 + w * 5120 + i * 1024);
    const int h = lane >> 5, m = lane & 31;
    u32x4 qf[2][2];
    float qn = 0.f;
    {
      f32x4 cs[3];
#pragma unroll
      for (int k = 0; k < 3; ++k) {
        const unsigned ca = lds0 + 21504 + (5 * w + (k < 2 ? 2 * k + h : 4)) * 16;
        cs[k] = ldsr<f32x4>(ca) + ldsr<f32x4>(ca + 320) + ldsr<f32x4>(ca + 640) + ldsr<f32x4>(ca + 960);
      }
#pragma unroll
      for (int b = 0; b < 2; ++b) {
        const int row = 32 * b + m;
        const f32x4 st = ldsr<f32x4>(lds0 + 20480 + row * 16);
        const float mean = (st[0] + st[2]) * (1.f / D_);
        float sc = __builtin_amdgcn_rsqf(__builtin_fmaxf((st[1] + st[3]) * (1.f / D_) - mean * mean, 0.f) + 1e-5f) * QSCALE;
        unsigned t[6];
#pragma unroll
        for (int k = 0; k < 3; ++k) {
          const unsigned ia = lds0 + 2 * 20480 + img_cell(row, 5 * w + (k < 2 ? 2 * k + h : 4));
          const f32x4 v = ldsr<f32x4>(ia) + ldsr<f32x4>(ia + 20480);
          t[2 * k] = cvtpk(__builtin_fmaf(-mean, cs[k][0], v[0]) * sc, __builtin_fmaf(-mean, cs[k][1], v[1]) * sc);
          t[2 * k + 1] = cvtpk(__builtin_fmaf(-mean, cs[k][2], v[2]) * sc, __builtin_fmaf(-mean, cs[k][3], v[3]) * sc);
        }
        if (h) { t[4] = 0u; t[5] = 0u; }
        qf[b][0] = u32x4{t[0], t[1], t[2], t[3]};
        qf[b][1] = u32x4{t[4], t[5], 0u, 0u};
        float n2 = 0.f;
#pragma unroll
        for (int i = 0; i < 6; ++i) { n2 = __builtin_fmaf(bflo(t[i]), bflo(t[i]), n2); n2 = __builtin_fmaf(bfhi(t[i]), bfhi(t[i]), n2); }
        n2 = x32_add(n2);
        if (!(n2 == n2)) n2 = __builtin_inff();
        qn = __builtin_fmaxf(qn, n2);
      }
    }
    qn = wave_max(qn);
    const float kn = wave_max(bflo(knb));
    const unsigned fx = lds0 + 3 * 20480;
    if (lane == 0) ldsw<u32x2>(fx + 80 * w, u32x2{f2u(qn), f2u(kn)});
    DRAIN_BARRIER();
#pragma unroll
    for (int i = 0; i < 5; ++i) glds16_sc1(kv + 20480 + w * 5120 + i * 1024, lane * 16, lds0 + 20480 + w * 5120 + i * 1024);
    const u32x2 f0 = ldsr<u32x2>(fx), f1 = ldsr<u32x2>(fx + 80), f2 = ldsr<u32x2>(fx + 160), f3 = ldsr<u32x2>(fx + 240);
    const float qm = __builtin_fmaxf(__builtin_fmaxf(u2f(f0[0]), u2f(f1[0])), __builtin_fmaxf(u2f(f2[0]), u2f(f3[0])));
    const float km = __builtin_fmaxf(__builtin_fmaxf(u2f(f0[1]), u2f(f1[1])), __builtin_fmaxf(u2f(f2[1]), u2f(f3[1])));
    const int slow = rfl(!(qm * km <= 3600.f));
    f32x16 oa, ob;
#pragma unroll
    for (int i = 0; i < 16; ++i) { oa[i] = 0.f; ob[i] = 0.f; }
    STAMP(5); STAMPC(28);
    if (!slow) {
      attn_fast(kv, lds0, w, lane, qf[0][0], qf[0][1], qf[1][0], qf[1][1], oa, ob);
    }
    else {
      attn_slow(rs, lane, qf[0][0], qf[0][1], oa);
      attn_slow(rs, lane, qf[1][0], qf[1][1], ob);
    }
    STAMP(6); STAMPC(29);
    const unsigned oi = lds0 + 2 * 20480;
    attn_stage(oi, 0, w, lane, oa);
    attn_stage(oi, 1, w, lane, ob);
    BARRIER();
#pragma unroll
    for (int r = 0; r < 3; ++r) {
      const int c = lane + 64 * r;
      if (r < 2 || lane < 32) bst16(rs, ldsr<u32x4>(oi + w * 2560 + c * 16), c * 16, WS_O + (4 * tg + w) * 20480 + hc * 2560);
    }
    publish(ws, WS_F3 + (tg * 8 + hc) * FS, tag);
    STAMP(7);
  } else {
    const int p = xcd * 32 + loc;
    const int pn = 4 * (xcd & 1) + (loc & 3), u = (xcd >> 1) + 4 * (loc >> 2);
    const int q4 = m16 >> 2, p4 = lane & 3;
    __builtin_amdgcn_s_setprio(3);
    u64 ep;
    asm volatile("global_load_dwordx2 %0, %1, %2 sc1" : "=v"(ep) : "v"(0u), "s"(ws) : "memory");
    u32x2 xr[5];
    {
      const __bf16* xp = x + (size_t)(8 * p + 2 * w + (lane >> 5)) * D_ + 4 * (lane & 31);
#pragma unroll
      for (int r = 0; r < 5; ++r) asm volatile("global_load_dwordx2 %0, %1, off offset:%2" : "=v"(xr[r]) : "v"(xp), "i"(256 * r) : "memory");
    }
    {
#pragma unroll
      for (int i = 0; i < 13; ++i) {
        const int pc = (4 * i + w + 3 * loc) % 50;
        glds16((pc < 25 ? (const char*)wk : (const char*)wv - 25600) + pc * 1024, lane * 16, lds0 + pc * 1024);
      }
    }
    asm volatile("s_waitcnt vmcnt(13)" : "+v"(ep), "+v"(xr[0]), "+v"(xr[1]), "+v"(xr[2]), "+v"(xr[3]), "+v"(xr[4])::"memory");
    STAMP(12);
    const u64 tag = mktag(ep);
    const unsigned xi = lds0 + 65536, pi = lds0 + 65536 + 10240;
    {
      float v[20], s = 0.f;
#pragma unroll
      for (int r = 0; r < 5; ++r) { v[4 * r] = bflo(xr[r][0]); v[4 * r + 1] = bfhi(xr[r][0]); v[4 * r + 2] = bflo(xr[r][1]); v[4 * r + 3] = bfhi(xr[r][1]); }
#pragma unroll
      for (int i = 0; i < 20; ++i) s += v[i];
      const float mean = x16_add(sum16(s)) * (1.f / D_);
      float s2 = 0.f;
#pragma unroll
      for (int i = 0; i < 20; ++i) { v[i] -= mean; s2 = __builtin_fmaf(v[i], v[i], s2); }
      const float rstd = __builtin_amdgcn_rsqf(x16_add(sum16(s2)) * (1.f / D_) + 1e-5f);
      const int i5 = lane & 31;
      const unsigned a = xi + (((i5 >> 3) * 4 + (i5 & 3)) * 8 + 2 * w + (lane >> 5)) * 16 + ((i5 >> 2) & 1) * 8;
#pragma unroll
      for (int r = 0; r < 5; ++r)
        ldsw<u32x2>(a + r * 2048, u32x2{cvtpk(v[4 * r] * rstd, v[4 * r + 1] * rstd), cvtpk(v[4 * r + 2] * rstd, v[4 * r + 3] * rstd)});
    }
    STAMP(1);
    DRAIN_BARRIER();
    STAMP(13);
    {
      const unsigned gb = WS_XN + (p >> 1) * 20480 + (p & 1) * 128;
#pragma unroll
      for (int r = 0; r < 3; ++r) {
        const int c = tid + 256 * r;
        if (r < 2 || tid < 128) bst16(rs, ldsr<u32x4>(xi + c * 16), gb + (c >> 3) * 256 + (c & 7) * 16, 0);
      }
    }
    {
      const unsigned r0 = lds0 + (w >> 1) * 25600 + (4 * g4 + q4) * 40 + ((w & 1) ? 32 : p4 * 8), b0 = xi + (g4 * 8 + (m16 & 7)) * 16;
      f32x4 r = {0.f, 0.f, 0.f, 0.f}, r1 = r;
      if (w < 2) {
#pragma unroll
        for (int s = 0; s < 20; s += 2) {
          r = mfma16(trfrag(r0 + s * 1280, 640), ldsr<u32x4>(b0 + s * 512), r);
          r1 = mfma16(trfrag(r0 + s * 1280 + 1280, 640), ldsr<u32x4>(b0 + s * 512 + 512), r1);
        }
      } else {
#pragma unroll
        for (int s = 0; s < 20; s += 2) {
          r = mfma16(ldsr<u32x4>(b0 + s * 512), trfrag(r0 + s * 1280, 640), r);
          r1 = mfma16(ldsr<u32x4>(b0 + s * 512 + 512), trfrag(r0 + s * 1280 + 1280, 640), r1);
        }
      }
      r += r1;
      STAMP(2);
      u32x2 v = {cvtpk(r[0], r[1]), cvtpk(r[2], r[3])};
      const unsigned kb = WS_KV + (p >> 2) * 2560;
      const int rho = 8 * (p & 3) + (m16 & 7);
      if (w < 2) {
        float n2 = bflo(v[0]) * bflo(v[0]) + bfhi(v[0]) * bfhi(v[0]) + bflo(v[1]) * bflo(v[1]) + bfhi(v[1]) * bfhi(v[1]);
        if (w == 0) n2 = x32_add(x16_add(n2));
        if (!(n2 == n2)) n2 = __builtin_inff();
        n2 = max16(n2);
        if (lane == 0) ldsw<float>(pi + 5120 + 4 * w, n2);
        if (w == 0) { if (m16 < 8) bst8(rs, v, kb + ((g4 & 1) * 32 + rho) * 16 + (g4 >> 1) * 8, 0); }
        else if (lane < 8) bst8(rs, v, kb + 1024 + rho * 8, 0);
      } else if (lane < 32 && (w == 2 || m16 < 4)) {
        bst8(rs, v, kb + 1280 + ((p >> 1) & 1) * 640 + (p & 1) * 320 + (16 * (w - 2) + m16) * 16 + g4 * 8, 0);
      }
    }
    DRAIN_BARRIER();
    if (tid == 0) {
      const unsigned kn = f2u(ldsr<float>(pi + 5120) + ldsr<float>(pi + 5124));
      const unsigned kb16 = kn >= 0x7f7f0000u ? 0x7f80u : (kn >> 16) + 1;
      __hip_atomic_store((GAS u64*)(ws + WS_F1 + p * FS), (tag & ~0xffffull) | kb16, RLX_AGENT);
    }
    const int c0 = ROT1 ? (loc + xcd) % 5 : 0;
    u32x4 wvo;
    const char *pg = nullptr, *pu = nullptr;
    if (u < 27) {
      const int sc = ((((lane & 7) >> 1) ^ ((lane >> 4) & 3)) << 1) | (lane & 1);
#pragma unroll
      for (int i = 0; i < 4; ++i) wvo[i] = (8 * i + (lane >> 3)) * (F_ * 2) + sc * 16;
      pg = (const char*)(wg + (size_t)(32 * w) * F_ + 64 * u);
      pu = (const char*)(wu + (size_t)(32 * w) * F_ + 64 * u);
#pragma unroll
      for (int i = 0; i < 4; ++i) glds16(pg + c0 * 442368, wvo[i], lds0 + w * 8192 + i * 1024);
#pragma unroll
      for (int i = 0; i < 4; ++i) glds16(pu + c0 * 442368, wvo[i], lds0 + w * 8192 + 4096 + i * 1024);
    }
    STAMP(3);
    if (u < 27) {
      poll(ws, WS_F1 + (32 * pn + 8 * w + (lane & 7)) * FS, tag);
      STAMP(4);
      u32x4 bvo, ra;
#pragma unroll
      for (int i = 0; i < 4; ++i) {
        bvo[i] = lane * 16 + i * 20480;
        ra[i] = lds0 + (4 * g4 + q4) * 128 + ((i ^ ((2 * g4 + (q4 >> 1)) & 3)) << 5) + p4 * 8;
      }
      f32x16 acc[8];
#pragma unroll
      for (int n = 0; n < 8; ++n)
#pragma unroll
        for (int i = 0; i < 16; ++i) acc[n][i] = 0.f;
      __builtin_amdgcn_s_setprio(GPRIO);
      STAMPC(26);
      gu_loop(pg, pu, ws + WS_XN + (16 * pn + 4 * w) * 20480, lds0 + w * 8192, bvo, wvo, ra, c0, acc);
      __builtin_amdgcn_s_setprio(3);
      STAMP(5); STAMPC(27);
#pragma unroll
      for (int tb = 0; tb < 4; ++tb)
#pragma unroll
        for (int k = 0; k < 2; ++k) {
          float hv[8];
#pragma unroll
          for (int i = 0; i < 8; ++i) hv[i] = silu_f(acc[2 * k + (i >> 2)][4 * tb + (i & 3)]) * acc[4 + 2 * k + (i >> 2)][4 * tb + (i & 3)];
          bst16(rs, u32x4{cvtpk(hv[0], hv[1]), cvtpk(hv[2], hv[3]), cvtpk(hv[4], hv[5]), cvtpk(hv[6], hv[7])}, lane * 16,
                WS_H + ((16 * pn + 4 * w + tb) * 54 + 2 * u + k) * 1024);
        }
    } else {
      const unsigned lw = lds0 + w * 8192;
      for (int id = (pn * 5 + (u - 27)) * 4 + w; id < 270; id += 160) {
        const int s = id / 5, fi = id % 5;
        const char* src = (const char*)(wd + (size_t)(32 * s) * D_ + 128 * fi);
#pragma unroll
        for (int i = 0; i < 8; ++i) glds16(src + i * (4 * D_ * 2), (lane >> 4) * (D_ * 2) + (lane & 15) * 16, lw + i * 1024);
        WAITV(0);
#pragma unroll
        for (int i = 0; i < 8; ++i) {
          const int nt = 8 * fi + i;
          bst16(rs, trfrag(lw + (4 * g4 + q4) * 256 + i * 32 + p4 * 8, 16 * 256), lane * 16, WS_WD + (((nt / 5) * 54 + s) * 5 + nt % 5) * 1024);
        }
        WAITL();
      }
    }
    publish(ws, WS_F2 + (pn * 32 + u) * FS, tag);
    STAMP(6);
    {
    int tid2 = TIDX; asm volatile("" : "+v"(tid2));
    const int tid = tid2, lane = tid & 63, g4 = lane >> 4, m16 = lane & 15, q4 = m16 >> 2, p4 = lane & 3;
    poll(ws, WS_F2 + ((lane < 40 ? lane / 5 : 0) * 32 + 27 + lane % 5) * FS, tag);
    poll(ws, WS_F2 + ((tg >> 2) * 32 + (lane & 31)) * FS, tag);
    STAMP(7); STAMPC(24);
    touch((const char*)wo + loc * 25600 + w * 6400, (lane < 50 ? lane : 49) * 128, lds0 + 81408);
    unsigned eo[3], el[3];
#pragma unroll
    for (int r = 0; r < 3; ++r) {
      const int c = r < 2 ? lane + 64 * r : 128 + (lane & 31), row = c / 10, k8 = c - 10 * row;
      eo[r] = ((64 * tg + 16 * w + row) * D_ + 80 * hc + 8 * k8) * 2;
      el[r] = img_cell(row, 2 * k8);
    }
    u32x4 hs[3];
    {
      const int s0 = 13 * w + (w < 2 ? w : 2), ns = w < 2 ? 14 : 13;
      const unsigned sa = WS_WD + (hc * 54 + s0) * 5120, sb = WS_H + (4 * tg * 54 + s0) * 1024;
      u32x4 a[4][5], b[4][4];
      f32x4 acc[5][4];
#pragma unroll
      for (int n = 0; n < 5; ++n)
#pragma unroll
        for (int tb = 0; tb < 4; ++tb) acc[n][tb] = f32x4{0.f, 0.f, 0.f, 0.f};
      unsigned bo[4];
#pragma unroll
      for (int tb = 0; tb < 4; ++tb) bo[tb] = lane * 16 + ((tb + w) & 3) * (54 * 1024);
      int jj = ROT2 ? (3 * (loc & 3) + 5 * hc) % 13 : 0;
      auto load = [&](int j) __attribute__((always_inline)) NOPK {
#pragma unroll
        for (int tb = 0; tb < 4; ++tb) b[j & 3][tb] = bld16(rs, bo[tb], sb + jj * 1024);
#pragma unroll
        for (int n = 0; n < 5; ++n) a[j & 3][n] = bld16(rs, lane * 16 + (n & 3) * 1024, sa + jj * 5120 + (n >> 2) * 4096);
        jj = jj + 1 == ns ? 0 : jj + 1;
        asm volatile("" : "+s"(jj));
      };
      __builtin_amdgcn_s_setprio(G2PRIO);
      {
      load(0); __builtin_amdgcn_sched_barrier(0); load(1); __builtin_amdgcn_sched_barrier(0);
      if (G2DEPTH > 2) { load(2); __builtin_amdgcn_sched_barrier(0); }
#pragma unroll
      for (int j = 0; j < 14; ++j) {
        if (j + G2DEPTH < 14) load(j + G2DEPTH);
        if (j < 13 || w < 2) {
#pragma unroll
          for (int n = 0; n < 5; ++n)
#pragma unroll
            for (int tb = 0; tb < 4; ++tb) acc[n][tb] = mfma16(a[j & 3][n], b[j & 3][tb], acc[n][tb]);
        }
#pragma unroll
        for (int k = 0; k < 9; ++k) { __builtin_amdgcn_sched_group_barrier(0x8, 2, 0); __builtin_amdgcn_sched_group_barrier(0x20, 1, 0); }
        __builtin_amdgcn_sched_barrier(0);
      }
      }
      __builtin_amdgcn_s_setprio(3);
      STAMP(8); STAMPC(25);
      red_spill(acc, lds0, w, lane);
      BARRIER();
      f32x4 hres[5];
      red_gather(acc, lds0, w, lane, hres);
      const unsigned pw = lds0 + w * 20480 + 15360;
#pragma unroll
      for (int n = 0; n < 5; ++n)
        ldsw<f32x4>(pw + img_cell(m16, 4 * n + g4), f32x4{silu_f(hres[n][0]), silu_f(hres[n][1]), silu_f(hres[n][2]), silu_f(hres[n][3])});
      WAITL();
#pragma unroll
      for (int r = 0; r < 3; ++r) {
        const f32x4 p0 = ldsr<f32x4>(pw + el[r]), p1 = ldsr<f32x4>(pw + (el[r] ^ 16));
        hs[r] = u32x4{cvtpk(p0[0], p0[1]), cvtpk(p0[2], p0[3]), cvtpk(p1[0], p1[1]), cvtpk(p1[2], p1[3])};
      }
    }
    BARRIER();
    const int th = w & 1, kh = w >> 1;
    const unsigned ra = lds0 + 2 * kh * 5120 + (4 * g4 + q4) * 160 + p4 * 8;
    int gk[5];
#pragma unroll
    for (int i = 0; i < 5; ++i) gk[i] = ROTO ? (i + (loc & 3) + hc) % 5 : i;
    u32x4 a0[6][5];
    {
      unsigned wv5[5];
      t80_offsets(wv5, lane);
      const char* wb = (const char*)(wo + (size_t)(320 * kh + 32 * th) * D_ + 80 * hc);
#pragma unroll
      for (int g = 0; g < 4; ++g) t80_stage(wb, gk[g], wv5, lds0 + g * 20480 + w * 5120);
      asm volatile("s_waitcnt vmcnt(5)\n\ts_barrier" ::: "memory");
#pragma unroll
      for (int j = 0; j < 6; ++j)
#pragma unroll
        for (int n = 0; n < 5; ++n) a0[j][n] = trfrag(ra + (j >> 1) * 20480 + (j & 1) * 5120 + n * 32, 16 * 160);
#pragma unroll
      for (int j = 0; j < 6; ++j) asm volatile("" : "+v"(a0[j][0]), "+v"(a0[j][1]), "+v"(a0[j][2]), "+v"(a0[j][3]), "+v"(a0[j][4]));
      BARRIER();
      t80_stage(wb, gk[4], wv5, lds0 + w * 5120);
    }
    DRAIN_BARRIER();
    STAMP(9);
    if (bid == 0) {
      poll(ws, WS_F2 + tid * FS, tag);
      poll(ws, WS_F3 + tid * FS, tag);
      BARRIER();
      if (tid == 0) __hip_atomic_store((GAS u64*)(ws + WS_E), tag, RLX_AGENT);
    } else {
      poll(ws, WS_F3 + (tg * 8 + (lane & 7)) * FS, tag);
    }
    STAMP(10);
    {
      u32x4 b[10][2], xv[3];
      f32x4 res[5][2];
      const unsigned so = WS_O + (4 * tg + 2 * th) * 20480 + kh * 10240;
#define OLD(s_) do { b[s_][0] = bld16(rs, lane * 16 + ((s_) & 1) * 1024, so + gk[(s_) >> 1] * 2048); \
                     b[s_][1] = bld16(rs, lane * 16 + 20480 + ((s_) & 1) * 1024, so + gk[(s_) >> 1] * 2048); } while (0)
#pragma unroll
      for (int s = 0; s < 4; ++s) { OLD(s); __builtin_amdgcn_sched_barrier(0); }
#pragma unroll
      for (int n = 0; n < 5; ++n) { res[n][0] = f32x4{0.f, 0.f, 0.f, 0.f}; res[n][1] = res[n][0]; }
      STAMP(14);
#pragma unroll
      for (int s = 0; s < 10; ++s) {
        if (s < 6) OLD(s + 4);
        if (s == 6) { xv[0] = *(const u32x4*)((const char*)x + eo[0]); xv[1] = *(const u32x4*)((const char*)x + eo[1]); }
        if (s == 7) xv[2] = *(const u32x4*)((const char*)x + eo[2]);
#pragma unroll
        for (int n = 0; n < 5; ++n) {
          res[n][0] = mfma16(a0[s % 6][n], b[s][0], res[n][0]);
          res[n][1] = mfma16(a0[s % 6][n], b[s][1], res[n][1]);
          if (s < 4) a0[s][n] = trfrag(ra + (((s + 6) >> 1) & 3) * 20480 + (s & 1) * 5120 + n * 32, 16 * 160);
        }
        __builtin_amdgcn_sched_group_barrier(0x8, 1, 0); __builtin_amdgcn_sched_group_barrier(0x20, 1, 0);
        __builtin_amdgcn_sched_group_barrier(0x8, 1, 0); __builtin_amdgcn_sched_group_barrier(0x20, 1, 0);
#pragma unroll
        for (int k = 0; k < 8; ++k) { __builtin_amdgcn_sched_group_barrier(0x100, 1, 0); __builtin_amdgcn_sched_group_barrier(0x8, 1, 0); }
        __builtin_amdgcn_sched_barrier(0);
      }
      STAMP(17);
#pragma unroll
      for (int t = 0; t < 2; ++t)
#pragma unroll
        for (int n = 0; n < 5; ++n) ldsw<f32x4>(lds0 + (1 + kh) * 20480 + img_cell(32 * th + 16 * t + m16, 4 * n + g4), res[n][t]);
      BARRIER();
      STAMP(18);
      const unsigned pw = lds0 + 20480 + w * 5120;
#pragma unroll
      for (int r = 0; r < 3; ++r) {
        const f32x4 p0 = ldsr<f32x4>(pw + el[r]) + ldsr<f32x4>(pw + 20480 + el[r]);
        const f32x4 p1 = ldsr<f32x4>(pw + (el[r] ^ 16)) + ldsr<f32x4>(pw + 20480 + (el[r] ^ 16));
        const u32x4 o4 = {cvtpk(__builtin_fmaf(p0[0], bflo(hs[r][0]), bflo(xv[r][0])), __builtin_fmaf(p0[1], bfhi(hs[r][0]), bfhi(xv[r][0]))),
                          cvtpk(__builtin_fmaf(p0[2], bflo(hs[r][1]), bflo(xv[r][1])), __builtin_fmaf(p0[3], bfhi(hs[r][1]), bfhi(xv[r][1]))),
                          cvtpk(__builtin_fmaf(p1[0], bflo(hs[r][2]), bflo(xv[r][2])), __builtin_fmaf(p1[1], bfhi(hs[r][2]), bfhi(xv[r][2]))),
                          cvtpk(__builtin_fmaf(p1[2], bflo(hs[r][3]), bflo(xv[r][3])), __builtin_fmaf(p1[3], bfhi(hs[r][3]), bfhi(xv[r][3])))};
        if (r < 2 || lane < 32) *(u32x4*)((char*)out + eo[r]) = o4;
      }
    }
    }
    STAMP(11);
  }
  STAMPC(31);
  STAMP_FLUSH();
}

extern "C" void kernel_launch(void* const* d_in, const int* in_sizes, int n_in, void* d_out, int out_size, void* d_ws, size_t ws_size,
                              hipStream_t stream) {
  (void)hipFuncSetAttribute((const void*)mega, hipFuncAttributeMaxDynamicSharedMemorySize, LDS_BYTES);
  mega<<<512, 256, LDS_BYTES, stream>>>((const __bf16*)d_in[0], (const __bf16*)d_in[1], (const __bf16*)d_in[2], (const __bf16*)d_in[3],
                                        (const __bf16*)d_in[4], (const __bf16*)d_in[6], (const __bf16*)d_in[7], (const __bf16*)d_in[8],
                                        (char*)d_ws, (__bf16*)d_out);
}
```
